```python
import torch

def _softmax(x, axis=-1):
    return torch.softmax(x, dim=axis)
def _gelu(x):
    return torch.nn.functional.gelu(x, approximate="tanh")
def _silu(x):
    return torch.nn.functional.silu(x)
def _sigmoid(x):
    return torch.sigmoid(x)
def _layernorm(x, axis=-1, eps=1e-5):
    m = x.mean(dim=axis, keepdim=True)
    v = x.var(dim=axis, keepdim=True, unbiased=False)
    return (x - m) / torch.sqrt(v + eps)
def _scatter_add(tgt, idx, src, axis=0):
    return tgt.clone().index_add_(axis, idx, src)
def _segment_sum(src, seg, n):
    out = torch.zeros((n,) + src.shape[1:], dtype=src.dtype)
    return out.index_add_(0, seg, src)
def _pad(x, pw):
    flat = tuple(v for pair in reversed(pw) for v in pair)
    return torch.nn.functional.pad(x, flat)

kernel_name = 'parallel_sum__grouped_kv_mqa__glu_gated_geglu__prompt'

def setup_inputs():

    _w = (lambda a: (lambda t: t.to(torch.bfloat16) if t.is_floating_point() else t)(torch.as_tensor(a)))
    def _A_setup():
        B, S, D, H = 1, 2048, 512, 32
        Dh = D // H
        return {
            "x": _w((lambda *s: torch.randn(*s, dtype=torch.bfloat16) * 0.05)(B, S, D)),
            "wq": _w((lambda *s: torch.randn(*s, dtype=torch.bfloat16) * 0.05)(D, H * Dh)),
            "wk": _w((lambda *s: torch.randn(*s, dtype=torch.bfloat16) * 0.05)(D, Dh)),
            "wv": _w((lambda *s: torch.randn(*s, dtype=torch.bfloat16) * 0.05)(D, Dh)),
            "wo": _w((lambda *s: torch.randn(*s, dtype=torch.bfloat16) * 0.05)(H * Dh, D)),
        }
    _A_in = _A_setup()

    def _B_setup():
        B, S, Dm, Df = 1, 2048, 512, 1792
        return {
            "x": _w((lambda *s: torch.randn(*s, dtype=torch.bfloat16) * 0.05)(B, S, Dm)),
            "w_gate": _w((lambda *s: torch.randn(*s, dtype=torch.bfloat16) * 0.4)(Dm, Df) * (Dm ** -0.5)),
            "w_up": _w((lambda *s: torch.randn(*s, dtype=torch.bfloat16) * 0.4)(Dm, Df) * (Dm ** -0.5)),
            "w_down": _w((lambda *s: torch.randn(*s, dtype=torch.bfloat16) * 0.4)(Df, Dm) * (Df ** -0.5)),
        }
    _B_in = _B_setup()

    return {
        "A_x": _A_in["x"],
        "A_wq": _A_in["wq"],
        "A_wk": _A_in["wk"],
        "A_wv": _A_in["wv"],
        "A_wo": _A_in["wo"],
        "B_x": _B_in["x"],
        "B_w_gate": _B_in["w_gate"],
        "B_w_up": _B_in["w_up"],
        "B_w_down": _B_in["w_down"],
    }

def reference(A_x, A_wq, A_wk, A_wv, A_wo, B_x, B_w_gate, B_w_up, B_w_down):
    def _A_ref(x, wq, wk, wv, wo):
        B, S, D = x.shape
        H = 32
        Dh = D // H
        q = (lambda x, *d: x.permute(*d))((x @ wq).reshape(B, S, H, Dh), 0, 2, 1, 3)
        k = x @ wk
        v = x @ wv
        scores = torch.einsum("bhqd,bkd->bhqk", q, k) * (Dh ** -0.5)
        p = _softmax(scores, axis=-1)
        o = torch.einsum("bhqk,bkd->bhqd", p, v)
        return (lambda x, *d: x.permute(*d))(o, 0, 2, 1, 3).reshape(B, S, H * Dh) @ wo

    def _B_ref(x, w_gate, w_up, w_down):
        g = torch.einsum("bsd,df->bsf", x, w_gate)
        u = torch.einsum("bsd,df->bsf", x, w_up)
        h = _gelu(g) * u
        return torch.einsum("bsf,fd->bsd", h, w_down)

    x = A_x
    xn = _layernorm(x, axis=-1)
    return x + _A_ref(xn, A_wq, A_wk, A_wv, A_wo) + _B_ref(xn, B_w_gate, B_w_up, B_w_down)
```

```cpp
#include <hip/hip_runtime.h>
#include <hip/hip_bf16.h>
#include <stdint.h>
typedef unsigned short u16;
__device__ __forceinline__ float bf2f(u16 b){ return __uint_as_float(((unsigned)b)<<16); }
__device__ __forceinline__ u16 f2bf(float f){ unsigned u=__float_as_uint(f); u += 0x7fffu + ((u>>16)&1u); return (u16)(u>>16); }
constexpr int S=2048, D=512, H=32, DH=16, F=1792;

__global__ void k_ln(const u16* x, u16* xn){
  int row=blockIdx.x; int t=threadIdx.x;
  float v[8]; float s=0;
  for(int i=0;i<8;++i){ v[i]=bf2f(x[row*D+t*8+i]); s+=v[i]; }
  for(int o=32;o;o>>=1) s+=__shfl_xor(s,o);
  float m=s*(1.f/D); float q=0;
  for(int i=0;i<8;++i){ float d=v[i]-m; q+=d*d; }
  for(int o=32;o;o>>=1) q+=__shfl_xor(q,o);
  float var=q*(1.f/D);
  float mb=bf2f(f2bf(m)); float vb=bf2f(f2bf(var)); float ve=bf2f(f2bf(vb+1e-5f)); float sd=bf2f(f2bf(sqrtf(ve)));
  for(int i=0;i<8;++i){ float d=bf2f(f2bf(v[i]-mb)); xn[row*D+t*8+i]=f2bf(d/sd); }
}
__global__ void k_gemm(const u16* A, const u16* B, u16* C, int M, int N, int K, int ldc){
  int n=blockIdx.x*64+threadIdx.x; int m=blockIdx.y;
  if(n>=N) return;
  float acc=0;
  for(int k=0;k<K;++k) acc+=bf2f(A[(size_t)m*K+k])*bf2f(B[(size_t)k*N+n]);
  C[(size_t)m*ldc+n]=f2bf(acc);
}
__global__ void k_attn(const u16* q, const u16* k, const u16* v, u16* o){
  int r=blockIdx.x*64+threadIdx.x;
  int t=r>>5,h=r&31;
  float qq[16]; for(int d=0;d<16;++d) qq[d]=bf2f(q[t*D+h*16+d]);
  float m=-1e30f,l=0; float acc[16]; for(int d=0;d<16;++d) acc[d]=0;
  for(int j=0;j<S;++j){ float s=0; for(int d=0;d<16;++d) s+=qq[d]*bf2f(k[j*16+d]); s*=0.25f;
    float mn=fmaxf(m,s); float a=__expf(m-mn); float p=__expf(s-mn); l=l*a+p; for(int d=0;d<16;++d) acc[d]=acc[d]*a+p*bf2f(v[j*16+d]); m=mn; }
  for(int d=0;d<16;++d) o[t*D+h*16+d]=f2bf(acc[d]/l);
}
__global__ void k_geglu(const u16* g, const u16* u, u16* h, int n){
  int i=blockIdx.x*256+threadIdx.x; if(i>=n) return;
  float x=bf2f(g[i]); float t=tanhf(0.7978845608f*(x+0.044715f*x*x*x)); float ge=bf2f(f2bf(0.5f*x*(1.f+t)));
  h[i]=f2bf(ge*bf2f(u[i]));
}
__global__ void k_sum(const u16* x,const u16* a,const u16* b,u16* o,int n){
  int i=blockIdx.x*256+threadIdx.x; if(i>=n) return;
  float t=bf2f(f2bf(bf2f(x[i])+bf2f(a[i]))); o[i]=f2bf(t+bf2f(b[i]));
}
extern "C" void kernel_launch(void* const* d_in, const int* in_sizes, int n_in, void* d_out, int out_size, void* d_ws, size_t ws_size, hipStream_t stream){
  const u16* x=(const u16*)d_in[0]; const u16* wq=(const u16*)d_in[1]; const u16* wk=(const u16*)d_in[2]; const u16* wv=(const u16*)d_in[3]; const u16* wo=(const u16*)d_in[4];
  const u16* wg=(const u16*)d_in[6]; const u16* wu=(const u16*)d_in[7]; const u16* wd=(const u16*)d_in[8];
  u16* ws=(u16*)d_ws;
  u16* xn=ws; u16* q=xn+S*D; u16* k=q+S*D; u16* v=k+S*16; u16* o=v+S*16; u16* g=o+S*D; u16* u=g+S*F; u16* h=u+S*F; u16* a=h+S*F; u16* b=a+S*D;
  k_ln<<<S,64,0,stream>>>(x,xn);
  k_gemm<<<dim3(8,S),64,0,stream>>>(xn,wq,q,S,D,D,D);
  k_gemm<<<dim3(1,S),64,0,stream>>>(xn,wk,k,S,16,D,16);
  k_gemm<<<dim3(1,S),64,0,stream>>>(xn,wv,v,S,16,D,16);
  k_attn<<<S*H/64,64,0,stream>>>(q,k,v,o);
  k_gemm<<<dim3(8,S),64,0,stream>>>(o,wo,a,S,D,D,D);
  k_gemm<<<dim3(F/64,S),64,0,stream>>>(xn,wg,g,S,F,D,F);
  k_gemm<<<dim3(F/64,S),64,0,stream>>>(xn,wu,u,S,F,D,F);
  k_geglu<<<S*F/256,256,0,stream>>>(g,u,h,S*F);
  k_gemm<<<dim3(8,S),64,0,stream>>>(h,wd,b,S,D,F,D);
  k_sum<<<S*D/256,256,0,stream>>>(x,a,b,(u16*)d_out,S*D);
}
```

```cpp
#include <hip/hip_runtime.h>
#include <stdint.h>
#pragma clang diagnostic ignored "-Wint-to-pointer-cast"

typedef unsigned short u16;
typedef unsigned long long u64;
typedef __bf16 bf16x2 __attribute__((ext_vector_type(2)));
typedef __bf16 bf16x8 __attribute__((ext_vector_type(8)));
typedef float f32x2 __attribute__((ext_vector_type(2)));
typedef float f32x4 __attribute__((ext_vector_type(4)));
typedef float f32x16 __attribute__((ext_vector_type(16)));
typedef unsigned u32x2 __attribute__((ext_vector_type(2)));
typedef unsigned u32x4 __attribute__((ext_vector_type(4)));
typedef short s16x4 __attribute__((ext_vector_type(4)));
typedef __attribute__((address_space(1))) u64 gu64;
#define LDSP(T) __attribute__((address_space(3))) T*
#define DI __device__ __forceinline__
#define RLX_AGENT __ATOMIC_RELAXED, __HIP_MEMORY_SCOPE_AGENT

constexpr int S_ = 2048, D_ = 512, F_ = 1792;
constexpr unsigned KF_OFF = 0;
constexpr unsigned VF_OFF = KF_OFF + S_ * 16 * 2;
constexpr unsigned OF_OFF = VF_OFF + S_ * 16 * 2;
constexpr unsigned HF_OFF = OF_OFF + S_ * D_ * 2;
constexpr unsigned ST_OFF = HF_OFF + S_ * F_ * 2;
constexpr unsigned CS_OFF = ST_OFF + S_ * 8;
constexpr unsigned PAY_END = CS_OFF + 2 * F_ * 4;
constexpr unsigned CTL_OFF = 16u << 20;
constexpr unsigned EPOCH_OFF = CTL_OFF;
constexpr unsigned KVF_OFF = CTL_OFF + 4096;
constexpr unsigned HFL_OFF = CTL_OFF + 8192;
constexpr unsigned OFL_OFF = CTL_OFF + 12288;
constexpr unsigned SEEN_OFF = CTL_OFF + 32768;
constexpr unsigned CSF_OFF = CTL_OFF + 40960;
constexpr int LDS_BYTES = 80 * 1024;
constexpr float QSCALE = 0.25f * 1.4426950408889634f;
constexpr int SPIN_MAX = 40000;
#define UP_DELAY 8

#define TS_DECL
#define TS(i)
#define TS_DUMP(kid)
#define TS_PARAM
#define TS_ARG

DI f32x16 mfma(u32x4 a, u32x4 b, f32x16 c) {
  return __builtin_amdgcn_mfma_f32_32x32x16_bf16(__builtin_bit_cast(bf16x8, a), __builtin_bit_cast(bf16x8, b), c, 0, 0, 0);
}
DI unsigned cvtpk(float lo, float hi) { f32x2 v = {lo, hi}; bf16x2 b = __builtin_convertvector(v, bf16x2); return __builtin_bit_cast(unsigned, b); }
DI float bfr(float f) { return __uint_as_float(cvtpk(f, f) << 16); }
DI float dot2(unsigned a, unsigned b, float c) { return __builtin_amdgcn_fdot2_f32_bf16(__builtin_bit_cast(bf16x2, a), __builtin_bit_cast(bf16x2, b), c, false); }
DI u32x4 lds128(unsigned addr) { return *(LDSP(const u32x4))(addr); }
DI f32x4 lds128f(unsigned addr) { return *(LDSP(const f32x4))(addr); }
DI f32x2 lds64f(unsigned addr) { return *(LDSP(const f32x2))(addr); }
DI float lds32f(unsigned addr) { return *(LDSP(const float))(addr); }
DI void sts128(unsigned addr, u32x4 v) { *(LDSP(u32x4))(addr) = v; }
DI void sts128f(unsigned addr, f32x4 v) { *(LDSP(f32x4))(addr) = v; }
DI void sts64f(unsigned addr, f32x2 v) { *(LDSP(f32x2))(addr) = v; }
DI void sts32f(unsigned addr, float v) { *(LDSP(float))(addr) = v; }
DI u32x2 ldstr(unsigned addr) { return __builtin_bit_cast(u32x2, __builtin_amdgcn_ds_read_tr16_b64_v4i16((LDSP(s16x4))(addr))); }
DI u32x4 ldstr2(unsigned a0, unsigned a1) { u32x2 lo = ldstr(a0), hi = ldstr(a1); return (u32x4){lo[0], lo[1], hi[0], hi[1]}; }
typedef __amdgpu_buffer_rsrc_t rsrc_t;
DI rsrc_t mkrs(const void* p, unsigned bytes) { return __builtin_amdgcn_make_buffer_rsrc((void*)p, 0, (int)bytes, 0x00020000); }
#define BDMA_(MOD, rs, voff, ldsw, lconst, gconst) do { unsigned t_; \
  asm volatile("s_add_u32 m0, %2, %4\n\ts_mov_b32 %0, %5\n\ts_nop 2\n\tbuffer_load_dwordx4 %1, %3, %0 offen " MOD "lds" \
               : "=&s"(t_) : "v"(voff), "s"(ldsw), "s"(rs), "i"(lconst), "i"(gconst) : "memory", "scc"); } while (0)
#define BDMA(rs, voff, ldsw, lconst, gconst) BDMA_("", rs, voff, ldsw, lconst, gconst)
#define BDMA_SC1(rs, voff, ldsw, lconst, gconst) BDMA_("sc1 ", rs, voff, ldsw, lconst, gconst)
DI void bdma_rt_sc1(rsrc_t rs, unsigned voff, unsigned lds, unsigned goff) {
  asm volatile("s_mov_b32 m0, %1\n\ts_nop 3\n\tbuffer_load_dwordx4 %0, %2, %3 offen sc1 lds" :: "v"(voff), "s"(lds), "s"(rs), "s"(goff) : "memory");
}
#define SBAR() __builtin_amdgcn_sched_barrier(0)
#define PIN(x) asm volatile("" : "+v"(x))
#define BAR() asm volatile("s_waitcnt lgkmcnt(0)\n\ts_barrier" ::: "memory")
#define DRAIN() asm volatile("s_waitcnt vmcnt(0)" ::: "memory")
template <int N> DI void waitv_bar() { asm volatile("s_waitcnt vmcnt(%0) lgkmcnt(0)\n\ts_barrier" :: "i"(N) : "memory"); }
template <int N> DI void waitv_bar_nl() { asm volatile("s_waitcnt vmcnt(%0)\n\ts_barrier" :: "i"(N) : "memory"); }
template <int C> DI float dppadd(float v) { return v + __uint_as_float(__builtin_amdgcn_update_dpp(0, __float_as_uint(v), C, 0xf, 0xf, true)); }
DI f32x16 zero16() { f32x16 z; for (int i = 0; i < 16; ++i) z[i] = 0.f; return z; }
DI float xhalf_add(float v) {
  auto r = __builtin_amdgcn_permlane32_swap(__float_as_uint(v), __float_as_uint(v), false, false);
  return __uint_as_float(r[0]) + __uint_as_float(r[1]);
}
DI float xhalf_max(float v) {
  auto r = __builtin_amdgcn_permlane32_swap(__float_as_uint(v), __float_as_uint(v), false, false);
  return fmaxf(__uint_as_float(r[0]), __uint_as_float(r[1]));
}
constexpr unsigned ONES2 = 0x3f803f80u;
DI float sum8(u32x4 v, float s) { s = dot2(v[0], ONES2, s); s = dot2(v[1], ONES2, s); s = dot2(v[2], ONES2, s); return dot2(v[3], ONES2, s); }
DI float sq8(u32x4 v, float s) { s = dot2(v[0], v[0], s); s = dot2(v[1], v[1], s); s = dot2(v[2], v[2], s); return dot2(v[3], v[3], s); }
DI u32x4 pack8f(f32x4 a, f32x4 b) { return (u32x4){cvtpk(a[0], a[1]), cvtpk(a[2], a[3]), cvtpk(b[0], b[1]), cvtpk(b[2], b[3])}; }
DI f32x4 q4(const f32x16& a, int g) { return (f32x4){a[4 * g], a[4 * g + 1], a[4 * g + 2], a[4 * g + 3]}; }
DI void st_sc1(char* ws, unsigned off, u32x4 v) {
  __amdgpu_buffer_rsrc_t rs = __builtin_amdgcn_make_buffer_rsrc(ws, 0, (int)PAY_END, 0x00020000);
  __builtin_amdgcn_raw_buffer_store_b128(v, rs, (int)off, 0, 16);
}
DI void set_tag(char* ws, unsigned off, u64 tag) { __hip_atomic_store((gu64*)(ws + off), tag, RLX_AGENT); }
DI void poll_tags(char* ws, unsigned off, bool active, u64 tag) {
#pragma unroll 1
  for (int it = 0; it < SPIN_MAX; ++it) {
    u64 v = tag;
    if (active) v = __hip_atomic_load((gu64*)(ws + off), RLX_AGENT);
    if (__all(v == tag)) break;
    __builtin_amdgcn_s_sleep(1);
  }
}

DI f32x2 ln_scalars(float s, float ss) {
  float m = s * (1.f / D_);
  float var = fmaxf(ss * (1.f / D_) - m * m, 0.f);
  float sd = bfr(__builtin_amdgcn_sqrtf(bfr(bfr(var) + 1e-5f)));
  return (f32x2){bfr(m), __builtin_amdgcn_rcpf(sd)};
}

DI u32x4 role_q(const u16* x, const u16* wq, int rb, int cb, int w, int lane TS_PARAM) {
  constexpr int NS = 5, SLOT = 16384;
  const int mt = w & 1, nt = (w >> 1) & 1, kh = w >> 2;
  const int hh = lane >> 5;
  const int drow = 8 * w + (lane >> 3), dpc = lane & 7;
  const unsigned xoff = (rb * 64 + drow) * 1024 + ((dpc ^ ((drow >> 1) & 7)) << 4);
  const unsigned woff = drow * 1024 + cb * 128 + ((dpc ^ (((drow >> 1) & 1) << 2)) << 4);
  const rsrc_t rx = mkrs(x, S_ * D_ * 2), rw = mkrs(wq, D_ * D_ * 2);
  const unsigned lw = w * 1024;
#define Q_ISSUE(j) do { BDMA(rx, xoff, lw, ((j) % NS) * SLOT, (j) * 128); BDMA(rw, woff, lw, ((j) % NS) * SLOT + 8192, (j) * 65536); } while (0)
#pragma unroll
  for (int j = 0; j < NS; ++j) Q_ISSUE(j);
  const int xr = 32 * mt + (lane & 31), sw = (xr >> 1) & 7;
  const unsigned xa0 = xr * 128 + (((2 * kh + hh) ^ sw) << 4);
  const unsigned xa1 = xr * 128 + (((2 * (kh + 2) + hh) ^ sw) << 4);
  const int r4 = (lane & 15) >> 2, p4 = lane & 3, grp = (lane >> 4) & 1;
  const unsigned wa = 8192 + (8 * hh + r4) * 128 + ((nt ^ (r4 >> 1)) << 6) + grp * 32 + p4 * 8 + kh * 2048;
  const unsigned sa = w * 1024 + lane * 16;
  f32x16 acc = zero16();
  float s1 = 0.f, s2 = 0.f, cs = 0.f;
#pragma unroll
  for (int j = 0; j < 8; ++j) {
    if (j == 0) waitv_bar<8>(); else if (j <= 3) waitv_bar<6>(); else if (j == 4) waitv_bar<6>(); else if (j == 5) waitv_bar<4>(); else if (j == 6) waitv_bar<2>(); else waitv_bar<0>();
    if (j == 0) TS(6);
    if (j == 7) TS(7);
    if (j >= 1 && j <= 3) Q_ISSUE(j + 4);
    const unsigned sb = (j % NS) * SLOT;
    u32x4 w0 = ldstr2(sb + wa, sb + wa + 512), x0 = lds128(sb + xa0);
    u32x4 w1 = ldstr2(sb + wa + 4096, sb + wa + 4096 + 512), x1 = lds128(sb + xa1);
    u32x4 xs = lds128(sb + sa);
    SBAR();
    acc = mfma(w0, x0, acc); cs = sum8(w0, cs); PIN(cs); SBAR();
    acc = mfma(w1, x1, acc); cs = sum8(w1, cs); s1 = sum8(xs, s1); s2 = sq8(xs, s2); PIN(cs); PIN(s1); PIN(s2); SBAR();
  }
  s1 = dppadd<0xB1>(s1); s2 = dppadd<0xB1>(s2);
  s1 = dppadd<0x4E>(s1); s2 = dppadd<0x4E>(s2);
  s1 = dppadd<0x141>(s1); s2 = dppadd<0x141>(s2);
  cs = xhalf_add(cs);
  const f32x2 st = ln_scalars(s1, s2);
  constexpr unsigned SC = 3 * SLOT, CSB = 4 * SLOT, STB = 4 * SLOT + 4096;
  if ((lane & 7) == 0) sts64f(STB + drow * 8, st);
  if (mt == 0 && lane < 32) sts32f(CSB + (kh * 64 + nt * 32 + lane) * 4, cs);
  f32x4 k0, k1;
  if (kh) { sts128f(SC + w * 2048 + lane * 16, q4(acc, 0)); sts128f(SC + w * 2048 + 1024 + lane * 16, q4(acc, 1)); k0 = q4(acc, 2); k1 = q4(acc, 3); }
  else    { sts128f(SC + w * 2048 + lane * 16, q4(acc, 2)); sts128f(SC + w * 2048 + 1024 + lane * 16, q4(acc, 3)); k0 = q4(acc, 0); k1 = q4(acc, 1); }
  BAR();
  const int pw = w ^ 4;
  k0 += lds128f(SC + pw * 2048 + lane * 16); k1 += lds128f(SC + pw * 2048 + 1024 + lane * 16);
  const unsigned ca = CSB + (nt * 32 + kh * 16 + 4 * hh) * 4;
  const f32x4 c0 = lds128f(ca) + lds128f(ca + 256), c1 = lds128f(ca + 32) + lds128f(ca + 256 + 32);
  const f32x2 rs = lds64f(STB + xr * 8);
  const float rq = rs[1] * QSCALE;
  return pack8f((k0 - rs[0] * c0) * rq, (k1 - rs[0] * c1) * rq);
}

template <int WHICH> DI void kv_loop(f32x16& acc, float& cs, float& s1, float& s2, unsigned xa0, unsigned xa1, unsigned wa, unsigned sa) {
#pragma unroll
  for (int J = 0; J < 4; ++J) {
    if (J == 0) waitv_bar<6>(); else if (J == 1) waitv_bar<4>(); else if (J == 2) waitv_bar<2>(); else waitv_bar<0>();
    const unsigned sb = J * 16384;
    u32x4 w0 = ldstr2(sb + wa, sb + wa + 128), x0 = lds128(sb + xa0);
    u32x4 w1 = ldstr2(sb + wa + 2048, sb + wa + 2048 + 128), x1 = lds128(sb + xa1);
    u32x4 xs = lds128(sb + sa);
    SBAR();
    acc = WHICH ? mfma(x0, w0, acc) : mfma(w0, x0, acc); cs = sum8(w0, cs); PIN(cs); SBAR();
    acc = WHICH ? mfma(x1, w1, acc) : mfma(w1, x1, acc); cs = sum8(w1, cs); s1 = sum8(xs, s1); s2 = sq8(xs, s2); PIN(cs); PIN(s1); PIN(s2); SBAR();
  }
}
DI void role_kv(const u16* x, const u16* wk, const u16* wv, char* ws, u64 tag, int gmt, int w, int lane TS_PARAM) {
  const int which = w & 1, kq = w >> 1, hh = lane >> 5;
  const int drow = 4 * w + (lane >> 4), dpc = lane & 15;
  const unsigned xoff = (gmt * 32 + drow) * 1024 + ((dpc ^ (drow & 15)) << 4);
  const rsrc_t rx = mkrs(x, S_ * D_ * 2), rw = mkrs(w < 4 ? wk : wv, D_ * 16 * 2);
  const unsigned woff = (w & 3) * 1024 + lane * 16, lw = w * 1024;
#pragma unroll
  for (int J = 0; J < 4; ++J) { BDMA(rx, xoff, lw, J * 16384, J * 256); BDMA(rw, woff, lw, J * 16384 + 8192, J * 4096); }
  const int xr = lane & 31;
  const unsigned xa0 = xr * 256 + (((2 * kq + hh) ^ (xr & 15)) << 4);
  const unsigned xa1 = xr * 256 + (((2 * (kq + 4) + hh) ^ (xr & 15)) << 4);
  const int r4 = (lane & 15) >> 2, p4 = lane & 3, grp = (lane >> 4) & 1;
  const unsigned wa = 8192 + (16 * kq + 8 * hh + r4) * 32 + grp * 4096 + p4 * 8;
  const unsigned sa = w * 1024 + lane * 16;
  f32x16 acc = zero16();
  float s1 = 0.f, s2 = 0.f, cs = 0.f;
  TS(1);
  if (which) kv_loop<1>(acc, cs, s1, s2, xa0, xa1, wa, sa); else kv_loop<0>(acc, cs, s1, s2, xa0, xa1, wa, sa);
  TS(2);
  s1 = dppadd<0xB1>(s1); s2 = dppadd<0xB1>(s2);
  s1 = dppadd<0x4E>(s1); s2 = dppadd<0x4E>(s2);
  s1 = dppadd<0x141>(s1); s2 = dppadd<0x141>(s2);
  s1 = dppadd<0x140>(s1); s2 = dppadd<0x140>(s2);
  cs = xhalf_add(cs);
  const f32x2 st = ln_scalars(s1, s2);
  constexpr unsigned STB = 65536, CSB = 65536 + 512;
  if ((lane & 15) == 0) { sts32f(STB + drow * 4, st[0]); sts32f(STB + 128 + drow * 4, st[1]); }
  if (which == 0 && lane < 32) sts32f(CSB + (kq * 32 + lane) * 4, cs);
  const unsigned EX = w * 4096 + lane * 16;
  sts128f(EX, q4(acc, 0)); sts128f(EX + 1024, q4(acc, 1));
  if (which) { sts128f(EX + 2048, q4(acc, 2)); sts128f(EX + 3072, q4(acc, 3)); }
  BAR();
  TS(3);
  if (w == 0) {
    f32x4 a0 = {0, 0, 0, 0}, a1 = a0, c0 = a0, c1 = a0;
#pragma unroll
    for (int k = 0; k < 4; ++k) {
      const unsigned e = k * 8192 + lane * 16, c = CSB + (k * 32 + 4 * hh) * 4;
      a0 += lds128f(e); a1 += lds128f(e + 1024); c0 += lds128f(c); c1 += lds128f(c + 32);
    }
    const float m = lds32f(STB + xr * 4), r = lds32f(STB + 128 + xr * 4);
    st_sc1(ws, KF_OFF + gmt * 1024 + lane * 16, pack8f((a0 - m * c0) * r, (a1 - m * c1) * r));
    DRAIN();
  } else if (w == 1 || w == 3) {
    const int s = w >> 1;
    f32x4 a0 = {0, 0, 0, 0}, a1 = a0; float c = 0.f;
#pragma unroll
    for (int k = 0; k < 4; ++k) {
      const unsigned e = k * 8192 + 4096 + s * 2048 + lane * 16;
      a0 += lds128f(e); a1 += lds128f(e + 1024); c += lds32f(CSB + (k * 32 + xr) * 4);
    }
    const unsigned sp = STB + (16 * s + 4 * hh) * 4;
    const f32x4 m0 = lds128f(sp), m1 = lds128f(sp + 32), r0 = lds128f(sp + 128), r1 = lds128f(sp + 160);
    const u32x4 o = pack8f((a0 - m0 * c) * r0, (a1 - m1 * c) * r1);
    if (xr >= 16) st_sc1(ws, VF_OFF + gmt * 1024 + s * 512 + hh * 256 + (lane & 15) * 16, o);
    DRAIN();
  } else if (w == 2) {
    const f32x2 m = lds64f(STB + (lane & 15) * 8), r = lds64f(STB + 128 + (lane & 15) * 8);
    if (lane < 16) st_sc1(ws, ST_OFF + gmt * 256 + lane * 16, __builtin_bit_cast(u32x4, (f32x4){m[0], r[0], m[1], r[1]}));
    DRAIN();
  }
  TS(4);
  BAR();
  TS(5);
  if (w == 0 && lane < 8) set_tag(ws, KVF_OFF + lane * 512 + gmt * 8, tag);
}

DI void role_cs(const u16* wm_, char* ws, u64 tag, int unit, int cb, int w, int lane) {
  const int tid = w * 64 + lane;
  const char* src = (const char*)wm_ + (size_t)(tid >> 4) * (F_ * 2) + cb * 256 + (tid & 15) * 16;
  u32x4 v[16];
#pragma unroll
  for (int i = 0; i < 16; ++i) v[i] = *(const u32x4*)(src + (size_t)i * 32 * (F_ * 2));
  float a[8] = {0, 0, 0, 0, 0, 0, 0, 0};
  unsigned sel_lo = 0x00003f80u;
  asm volatile("" : "+s"(sel_lo));
#pragma unroll
  for (int i = 0; i < 16; ++i)
#pragma unroll
    for (int k = 0; k < 4; ++k) { a[2 * k] = dot2(v[i][k], sel_lo, a[2 * k]); a[2 * k + 1] = dot2(v[i][k], 0x3f800000u, a[2 * k + 1]); }
  sts128f(tid * 32, (f32x4){a[0], a[1], a[2], a[3]}); sts128f(tid * 32 + 16, (f32x4){a[4], a[5], a[6], a[7]});
  BAR();
  if (w == 0) {
    f32x4 t = {0, 0, 0, 0};
#pragma unroll
    for (int i = 0; i < 16; ++i) t += lds128f((2 * i + (lane >> 5)) * 512 + (lane & 31) * 16);
#pragma unroll
    for (int i = 0; i < 4; ++i) t[i] = xhalf_add(t[i]);
    if (lane < 32) st_sc1(ws, CS_OFF + ((unit & 1) * F_ + cb * 128) * 4 + lane * 16, __builtin_bit_cast(u32x4, t));
    DRAIN();
    if (lane == 0) set_tag(ws, CSF_OFF + ((unit & 1) * 14 + cb) * 128, tag);
  }
  BAR();
}

DI void role_up(const u16* x, const u16* wg, const u16* wu, char* ws, u64 tag, int rb, int cb, int rep, int w, int lane TS_PARAM) {
  constexpr int NS = 3, SLOT = 24576, NST = 16;
  const int wm = w & 1, wn = w >> 1, hh = lane >> 5;
  const int arow = 16 * w + (lane >> 2);
  const unsigned aoff = (rb * 128 + arow) * 1024 + (((lane & 3) ^ ((arow >> 2) & 3)) << 4);
  const int brow = 4 * w + (lane >> 4);
  const unsigned boff = brow * (F_ * 2) + cb * 256 + (((lane & 15) ^ ((brow & 3) << 2)) << 4);
  const rsrc_t rx = mkrs(x, S_ * D_ * 2), rg = mkrs(wg, D_ * F_ * 2), ru = mkrs(wu, D_ * F_ * 2);
  const unsigned lw = w * 1024;
#define UP_A(s) BDMA(rx, aoff, lw, ((s) % NS) * SLOT, (s) * 64)
#define UP_G(s) BDMA(rg, boff, lw, ((s) % NS) * SLOT + 8192, (s) * 32 * F_ * 2)
#define UP_U(s) BDMA(ru, boff, lw, ((s) % NS) * SLOT + 16384, (s) * 32 * F_ * 2)
#pragma unroll
  for (int s = 0; s < NS; ++s) { UP_A(s); UP_G(s); UP_U(s); }
  const int r4 = (lane & 15) >> 2, p4 = lane & 3, grp = (lane >> 4) & 1;
  const unsigned wb = 8192 + (8 * hh + r4) * 256 + ((wn ^ r4) << 6) + grp * 32 + p4 * 8;
  const unsigned pa = wb, qa = wb + 8192;
  const int xr = 64 * wm + (lane & 31), sw = (xr >> 2) & 3;
  const unsigned xa0 = xr * 64 + ((hh ^ sw) << 4), xa1 = xr * 64 + (((2 + hh) ^ sw) << 4);
  constexpr unsigned STB = NS * SLOT, CSB = STB + 1024;
  f32x16 ap0 = zero16(), ap1 = zero16(), aq0 = zero16(), aq1 = zero16();
  u32x4 Ax0, Ax1, Ap, Aq, Bx0, Bx1, Bp, Bq;
#define UP_RD(X0, X1, P, Q, sb, xa, ko) do { P = ldstr2((sb) + pa + (ko), (sb) + pa + (ko) + 1024); X0 = lds128((sb) + (xa)); X1 = lds128((sb) + (xa) + 2048); \
    Q = ldstr2((sb) + qa + (ko), (sb) + qa + (ko) + 1024); } while (0)
  waitv_bar<6>();
  TS(1);
  UP_RD(Ax0, Ax1, Ap, Aq, 0u, xa0, 0);
#pragma unroll
  for (int s = 0; s < NST; ++s) {
    const unsigned sb = (s % NS) * SLOT, sn = ((s + 1) % NS) * SLOT;
    SBAR();
    UP_RD(Bx0, Bx1, Bp, Bq, sb, xa1, 4096);
    SBAR();
    ap0 = mfma(Ap, Ax0, ap0); ap1 = mfma(Ap, Ax1, ap1); aq0 = mfma(Aq, Ax0, aq0); aq1 = mfma(Aq, Ax1, aq1); SBAR();
    if (s + 1 < NST) {
      if (s + 2 < NST) waitv_bar<3>(); else waitv_bar<0>();
      UP_RD(Ax0, Ax1, Ap, Aq, sn, xa0, 0);
    }
    SBAR();
    ap0 = mfma(Bp, Bx0, ap0); SBAR();
    if (s + 3 < NST) UP_A(s + 3);
    ap1 = mfma(Bp, Bx1, ap1); SBAR();
    if (s + 3 < NST) UP_G(s + 3);
    aq0 = mfma(Bq, Bx0, aq0); SBAR();
    if (s + 3 < NST) UP_U(s + 3);
    aq1 = mfma(Bq, Bx1, aq1);
    asm volatile("" : "+v"(ap0), "+v"(ap1), "+v"(aq0), "+v"(aq1));
    SBAR();
    if (s == NST - 3 && w == 0) {
      poll_tags(ws, lane < 4 ? KVF_OFF + rep * 512 + (rb * 4 + lane) * 8 : CSF_OFF + ((lane - 4) * 14 + cb) * 128, lane < 6, tag);
      const rsrc_t rws = mkrs(ws, PAY_END);
      BDMA_SC1(rws, rb * 1024 + lane * 16, lw, STB, ST_OFF);
      BDMA_SC1(rws, cb * 512 + (lane >> 5) * (F_ * 4) + (lane & 31) * 16, lw, CSB, CS_OFF);
    }
  }
  TS(2);
  __builtin_amdgcn_s_setprio(3);
  constexpr float GA = -2.f * 0.7978845608028654f * 1.4426950408889634f, GB = GA * 0.044715f;
  const unsigned cpa = CSB + (wn * 32 + 4 * hh) * 4;
#pragma unroll
  for (int m = 0; m < 2; ++m) {
    const f32x16& P = m ? ap1 : ap0; const f32x16& Q = m ? aq1 : aq0;
    const f32x2 rs = lds64f(STB + (xr + 32 * m) * 8);
    const float nm = -rs[0], r2 = rs[1] * rs[1], ca = rs[1] * GA, cbb = r2 * rs[1] * GB, ir2 = __builtin_amdgcn_rcpf(r2);
    u32x4 o[2];
#pragma unroll
    for (int g = 0; g < 4; ++g) {
      const f32x4 cg = lds128f(cpa + g * 32), cu = lds128f(cpa + 512 + g * 32);
#pragma unroll
      for (int i = 0; i < 2; ++i) {
        const f32x2 gg = __builtin_elementwise_fma((f32x2){cg[2 * i], cg[2 * i + 1]}, (f32x2){nm, nm}, (f32x2){P[4 * g + 2 * i], P[4 * g + 2 * i + 1]});
        const f32x2 uu = __builtin_elementwise_fma((f32x2){cu[2 * i], cu[2 * i + 1]}, (f32x2){nm, nm}, (f32x2){Q[4 * g + 2 * i], Q[4 * g + 2 * i + 1]});
        const f32x2 z = gg * __builtin_elementwise_fma(gg * gg, (f32x2){cbb, cbb}, (f32x2){ca, ca});
        const f32x2 e = {__builtin_amdgcn_exp2f(z[0]), __builtin_amdgcn_exp2f(z[1])};
        const f32x2 dn = __builtin_elementwise_fma(e, (f32x2){ir2, ir2}, (f32x2){ir2, ir2});
        const f32x2 h = gg * uu * (f32x2){__builtin_amdgcn_rcpf(dn[0]), __builtin_amdgcn_rcpf(dn[1])};
        o[g >> 1][(g & 1) * 2 + i] = cvtpk(h[0], h[1]);
      }
    }
    const unsigned dst = HF_OFF + ((rb * 4 + wm * 2 + m) * 112 + cb * 8 + wn * 2) * 1024 + lane * 16;
    st_sc1(ws, dst, o[0]); st_sc1(ws, dst + 1024, o[1]);
  }
  __builtin_amdgcn_s_setprio(0);
  DRAIN();
  BAR();
  if (w == 0 && lane == 0) set_tag(ws, HFL_OFF + rb * 128 + cb * 8, tag);
  TS(3);
}

DI void role_down(const u16* x, const u16* wd, const u16* wo, u16* out, char* ws, u64 tag, int rb, int cb, int w, int lane TS_PARAM) {
  constexpr int NS = 5, SLOT = 16384, NH = 28;
  const int nh = w & 1, kq = w >> 1, hh = lane >> 5;
  const unsigned hoff = ((rb * 2 + (w >> 2)) * 112 + (w & 3)) * 1024 + lane * 16;
  const unsigned ooff = ((rb * 2 + (w >> 2)) * 32 + (w & 3)) * 1024 + lane * 16;
  const int brow = 8 * w + (lane >> 3);
  const unsigned boff = brow * 1024 + cb * 128 + (((lane & 7) ^ (((brow >> 1) & 1) << 2)) << 4);
  const rsrc_t rws = mkrs(ws, PAY_END), rd = mkrs(wd, F_ * D_ * 2), ro = mkrs(wo, D_ * D_ * 2);
  const unsigned lw = w * 1024;
#define DN_A(s) BDMA_SC1(rws, hoff, lw, ((s) % NS) * SLOT, HF_OFF + (s) * 4096)
#define DN_B(s) BDMA(rd, boff, lw, ((s) % NS) * SLOT + 8192, (s) * 65536)
#pragma unroll
  for (int s = 0; s < 4; ++s) DN_B(s);
  const int tid = w * 64 + lane, orow = tid >> 3, oc = tid & 7;
  const size_t go = ((size_t)(rb * 64 + orow) * D_ + cb * 64 + oc * 8);
  const u32x4 xr = *(const u32x4*)(x + go);
  if (w == 0) poll_tags(ws, HFL_OFF + (rb >> 1) * 128 + lane * 8, lane < 14, tag);
  BAR();
  TS(4);
#pragma unroll
  for (int s = 0; s < 4; ++s) DN_A(s);
  const int r4 = (lane & 15) >> 2, p4 = lane & 3, grp = (lane >> 4) & 1;
  const unsigned wa = (16 * kq + 4 * hh + r4) * 128 + ((nh ^ (r4 >> 1)) << 6) + grp * 32 + p4 * 8;
  const unsigned xa = kq * 1024 + lane * 16;
  f32x16 a0 = zero16(), a1 = zero16();
  u32x4 x0[2], x1[2], wf[2];
  waitv_bar<3>();
  wf[0] = ldstr2(8192 + wa, 8192 + wa + 1024); x0[0] = lds128(xa); x1[0] = lds128(xa + 4096);
#pragma unroll
  for (int s = 0; s < NH; ++s) {
    if (s + 1 < NH) {
      if (s == 0) waitv_bar_nl<2>(); else if (s == 1) waitv_bar_nl<3>(); else if (s == 2) waitv_bar_nl<4>();
      else if (s + 3 < NH) waitv_bar_nl<4>(); else if (s + 2 < NH) waitv_bar_nl<2>(); else waitv_bar_nl<0>();
      if (s + 4 < NH) { DN_A(s + 4); DN_B(s + 4); }
      const unsigned sn = ((s + 1) % NS) * SLOT;
      wf[(s + 1) & 1] = ldstr2(sn + 8192 + wa, sn + 8192 + wa + 1024); x0[(s + 1) & 1] = lds128(sn + xa); x1[(s + 1) & 1] = lds128(sn + xa + 4096);
    }
    SBAR();
    a0 = mfma(wf[s & 1], x0[s & 1], a0); a1 = mfma(wf[s & 1], x1[s & 1], a1);
    asm volatile("" : "+v"(a0), "+v"(a1));
    SBAR();
  }
  TS(5);
  BAR();
#pragma unroll
  for (int j = 0; j < 8; ++j) BDMA(ro, boff, lw, j * 8192, j * 65536);
  waitv_bar<0>();
  u32x4 wof[8];
#pragma unroll
  for (int j = 0; j < 8; ++j) wof[j] = ldstr2(j * 8192 + wa, j * 8192 + wa + 1024);
  if (w == 0) { asm volatile("s_waitcnt lgkmcnt(0)" ::: "memory"); __builtin_amdgcn_s_setprio(3); poll_tags(ws, OFL_OFF + rb * 512 + lane * 8, true, tag); }
  BAR();
  __builtin_amdgcn_s_setprio(3);
  TS(6);
#pragma unroll
  for (int j = 0; j < 8; ++j) BDMA_SC1(rws, ooff, lw, j * 8192, OF_OFF + j * 4096);
#pragma unroll
  for (int jj = 0; jj < 4; ++jj) {
    if (jj == 0) waitv_bar_nl<6>(); else if (jj == 1) waitv_bar_nl<4>(); else if (jj == 2) waitv_bar_nl<2>(); else waitv_bar_nl<0>();
    const int j = 2 * jj;
    const u32x4 p0 = lds128(j * 8192 + xa), p1 = lds128(j * 8192 + xa + 4096), p2 = lds128((j + 1) * 8192 + xa), p3 = lds128((j + 1) * 8192 + xa + 4096);
    a0 = mfma(wof[j], p0, a0); a1 = mfma(wof[j], p1, a1); a0 = mfma(wof[j + 1], p2, a0); a1 = mfma(wof[j + 1], p3, a1);
    SBAR();
  }
  BAR();
  constexpr unsigned PS = 64 * 272;
#pragma unroll
  for (int m = 0; m < 2; ++m) {
    const f32x16& a = m ? a1 : a0;
    const unsigned base = kq * PS + (m * 32 + (lane & 31)) * 272 + (nh * 32 + 4 * hh) * 4;
#pragma unroll
    for (int g = 0; g < 4; ++g) sts128f(base + g * 32, q4(a, g));
  }
  BAR();
  f32x4 s0 = {__uint_as_float(xr[0] << 16), __uint_as_float(xr[0] & 0xffff0000u), __uint_as_float(xr[1] << 16), __uint_as_float(xr[1] & 0xffff0000u)};
  f32x4 s1 = {__uint_as_float(xr[2] << 16), __uint_as_float(xr[2] & 0xffff0000u), __uint_as_float(xr[3] << 16), __uint_as_float(xr[3] & 0xffff0000u)};
#pragma unroll
  for (int k = 0; k < 4; ++k) { const unsigned b = k * PS + orow * 272 + oc * 32; s0 += lds128f(b); s1 += lds128f(b + 16); }
  *(u32x4*)(out + go) = pack8f(s0, s1);
  TS(7);
}

DI u32x4 pack8(const f32x16& p, int b) {
  return (u32x4){cvtpk(p[b], p[b + 1]), cvtpk(p[b + 2], p[b + 3]), cvtpk(p[b + 4], p[b + 5]), cvtpk(p[b + 6], p[b + 7])};
}
DI u32x4 ld_sc1(char* ws, unsigned off) {
  __amdgpu_buffer_rsrc_t rs = __builtin_amdgcn_make_buffer_rsrc(ws, 0, (int)PAY_END, 0x00020000);
  return __builtin_amdgcn_raw_buffer_load_b128(rs, (int)off, 0, 16);
}
DI void role_attn(char* ws, u64 tag, const u32x4 qf, int rb, int cb, int rep, int w, int lane TS_PARAM) {
  constexpr int NS = 4, SLOT = 16640, VT = 1056;
  const int gmt = rb * 2 + (w & 1), head = cb * 4 + ((w >> 1) & 1) * 2 + (w >> 2);
  const int hh = lane >> 5, d = lane & 31;
  const unsigned loff = lane * 16;
  const rsrc_t rws = mkrs(ws, PAY_END);
  auto issue = [&](int c) {
    const unsigned slot = (c % NS) * SLOT;
    bdma_rt_sc1(rws, loff, slot + w * 1024, KF_OFF + c * 8192 + w * 1024);
    bdma_rt_sc1(rws, loff, slot + 8192 + w * VT, VF_OFF + c * 8192 + w * 1024);
  };
  if (w == 0) poll_tags(ws, KVF_OFF + rep * 512 + lane * 8, true, tag);
  BAR();
  TS(2);
  issue(0); issue(1); issue(2);
  if (w == 0) {
    const int t = lane >> 1; const unsigned v = (lane & 1) ? 0u : ONES2;
    sts128((t >> 3) * SLOT + 8192 + (t & 7) * VT + 1024 + (lane & 1) * 16, (u32x4){v, v, v, v});
  }
  const unsigned va0 = 8192 + (d < 16 ? hh * 256 + d * 16 : 1024 + ((d == 16 || d == 20) ? 0 : 16));
  const unsigned va1 = va0 + (d < 16 ? 512 : 0);
  f32x16 o = zero16(), negm, sA, sB;
  waitv_bar<4>();
  u32x4 kf, v0, v1, pl, ph;
  {
    u32x4 k0 = lds128(loff);
    kf = lds128(loff + 1024);
    v0 = lds128(va0);
    v1 = (u32x4){0, 0, 0, 0}; ph = (u32x4){0, 0, 0, 0};
    f32x16 s = mfma(k0, qf, zero16());
    float m = fmaxf(s[0], s[1]);
#pragma unroll
    for (int i = 2; i < 16; i += 2) m = fmaxf(fmaxf(m, s[i]), s[i + 1]);
    m = xhalf_max(m);
#pragma unroll
    for (int i = 0; i < 16; ++i) { negm[i] = -m; sA[i] = s[i] - m; }
  }
  TS(3);
#define EX(i) C[i] = __builtin_amdgcn_exp2f(C[i])
#define ATILE(CUR, NXT, KN, V1A, V0N) do { f32x16& C = CUR; \
    NXT = mfma(kf, qf, negm); SBAR(); kf = lds128(KN); SBAR(); \
    EX(0); EX(1); EX(2); EX(3); pl[0] = cvtpk(C[0], C[1]); EX(4); EX(5); SBAR(); \
    o = mfma(v1, ph, o); SBAR(); v1 = lds128(V1A); SBAR(); \
    EX(6); pl[1] = cvtpk(C[2], C[3]); EX(7); pl[2] = cvtpk(C[4], C[5]); EX(8); pl[3] = cvtpk(C[6], C[7]); SBAR(); EX(9); EX(10); SBAR(); \
    o = mfma(v0, pl, o); SBAR(); v0 = lds128(V0N); SBAR(); \
    EX(11); EX(12); EX(13); EX(14); EX(15); SBAR(); ph[0] = cvtpk(C[8], C[9]); ph[1] = cvtpk(C[10], C[11]); ph[2] = cvtpk(C[12], C[13]); ph[3] = cvtpk(C[14], C[15]); SBAR(); \
  } while (0)
  for (int c = 0; c < 8; ++c) {
    const unsigned sb = (c & 3) * SLOT, sn = ((c + 1) & 3) * SLOT;
    unsigned ka = sb + loff, kn = sn + loff, a0 = sb + va0, a1 = sb + va1, an = sn + va0;
    asm volatile("" : "+v"(ka), "+v"(kn), "+v"(a0), "+v"(a1), "+v"(an));
    SBAR();
    ATILE(sA, sB, ka + 2 * 1024, a1 + 0 * VT, a0 + 1 * VT);
    ATILE(sB, sA, ka + 3 * 1024, a1 + 1 * VT, a0 + 2 * VT);
    ATILE(sA, sB, ka + 4 * 1024, a1 + 2 * VT, a0 + 3 * VT);
    ATILE(sB, sA, ka + 5 * 1024, a1 + 3 * VT, a0 + 4 * VT);
    if (c < 7) { if (c < 6) waitv_bar_nl<2>(); else waitv_bar_nl<0>(); }
    if (c < 5) issue(c + 3);
    SBAR();
    ATILE(sA, sB, ka + 6 * 1024, a1 + 4 * VT, a0 + 5 * VT);
    ATILE(sB, sA, ka + 7 * 1024, a1 + 5 * VT, a0 + 6 * VT);
    ATILE(sA, sB, kn, a1 + 6 * VT, a0 + 7 * VT);
    ATILE(sB, sA, kn + 1024, a1 + 7 * VT, an);
  }
  o = mfma(v1, ph, o);
  TS(4);
  float l = o[8];
  bool bad = !(l <= 1.8446744e19f);
#pragma unroll
  for (int i = 0; i < 8; ++i) bad |= !(__builtin_fabsf(o[i]) <= 3.0e38f);
  if (__any(bad)) {
    o = zero16();
    float m = -__builtin_inff();
    const unsigned cv = (d == 16 || d == 20) ? ONES2 : 0u;
    for (int t = 0; t < 64; ++t) {
      u32x4 kq = ld_sc1(ws, KF_OFF + t * 1024 + loff);
      u32x4 u0 = ld_sc1(ws, VF_OFF + t * 1024 + hh * 256 + (d & 15) * 16), u1 = ld_sc1(ws, VF_OFF + t * 1024 + 512 + hh * 256 + (d & 15) * 16);
      if (d >= 16) { u0 = (u32x4){cv, cv, cv, cv}; u1 = u0; }
      f32x16 s = mfma(kq, qf, zero16());
      float tm = fmaxf(s[0], s[1]);
#pragma unroll
      for (int i = 2; i < 16; i += 2) tm = fmaxf(fmaxf(tm, s[i]), s[i + 1]);
      tm = xhalf_max(tm);
      const float mn = fmaxf(m, tm);
      const float al = __builtin_amdgcn_exp2f(m - mn);
      m = mn;
      f32x16 p;
#pragma unroll
      for (int i = 0; i < 16; ++i) { p[i] = __builtin_amdgcn_exp2f(s[i] - mn); o[i] *= al; }
      o = mfma(u0, pack8(p, 0), o);
      o = mfma(u1, pack8(p, 8), o);
    }
    l = o[8];
  }
  const float inv = __builtin_amdgcn_rcpf(l);
  st_sc1(ws, OF_OFF + (gmt * 32 + head) * 1024 + loff, pack8f(q4(o, 0) * inv, q4(o, 1) * inv));
  DRAIN();
  if (lane == 0) set_tag(ws, OFL_OFF + rb * 512 + (cb * 8 + w) * 8, tag);
  TS(5);
}

__global__ void __launch_bounds__(512, 4) k_all(const u16* x, const u16* wq, const u16* wk, const u16* wv, const u16* wo,
                                                const u16* wg, const u16* wu, const u16* wd, u16* out, char* ws) {
  const int lane = threadIdx.x & 63, w = __builtin_amdgcn_readfirstlane(threadIdx.x >> 6);
  const int b = blockIdx.x, xcd = b & 7, j = (b >> 3) & 31;
  TS_DECL; TS(0);
  const u64 tag = *(const __attribute__((address_space(4))) u64*)(ws + EPOCH_OFF) + 1;
  if (b >= 256) {
    __builtin_amdgcn_s_setprio(3);
    const int rb = xcd * 4 + (j >> 3), cb = j & 7;
    const u32x4 qf = role_q(x, wq, rb, cb, w, lane TS_ARG);
    if (w == 1 && lane == 0) set_tag(ws, SEEN_OFF + b * 8, tag);
    TS(1);
    __builtin_amdgcn_s_setprio(2);
    role_attn(ws, tag, qf, rb, cb, xcd, w, lane TS_ARG);
    TS_DUMP(0);
  } else {
    if (w == 1 && lane == 0) set_tag(ws, SEEN_OFF + b * 8, tag);
    if (j < 8) {
      __builtin_amdgcn_s_setprio(3);
      role_kv(x, wk, wv, ws, tag, xcd * 8 + j, w, lane TS_ARG);
      TS_DUMP(2);
      __builtin_amdgcn_s_setprio(0);
    } else if (UP_DELAY) {
      for (int i = 0; i < UP_DELAY; ++i) __builtin_amdgcn_s_sleep(8);
    }
    if (j >= 4) role_up(x, wg, wu, ws, tag, (xcd >> 1) * 4 + ((j - 4) & 3), (xcd & 1) * 7 + ((j - 4) >> 2), xcd, w, lane TS_ARG);
    else {
      const int unit = (xcd >> 1) * 4 + j;
      if (unit < 14) role_cs((unit & 1) ? wu : wg, ws, tag, unit, (xcd & 1) * 7 + (unit >> 1), w, lane);
      else if (b == 31) {
        poll_tags(ws, SEEN_OFF + (w * 64 + lane) * 8, true, tag);
        BAR();
        if (w == 0 && lane == 0) set_tag(ws, EPOCH_OFF, tag);
      }
    }
    role_down(x, wd, wo, out, ws, tag, xcd * 4 + (j >> 3), j & 7, w, lane TS_ARG);
    TS_DUMP(1);
  }
}

extern "C" void kernel_launch(void* const* d_in, const int* in_sizes, int n_in, void* d_out, int out_size, void* d_ws, size_t ws_size, hipStream_t stream) {
  (void)hipFuncSetAttribute((const void*)k_all, hipFuncAttributeMaxDynamicSharedMemorySize, LDS_BYTES);
  k_all<<<512, 512, LDS_BYTES, stream>>>((const u16*)d_in[0], (const u16*)d_in[1], (const u16*)d_in[2], (const u16*)d_in[3], (const u16*)d_in[4],
                                         (const u16*)d_in[6], (const u16*)d_in[7], (const u16*)d_in[8], (u16*)d_out, (char*)d_ws);
}
```

```cpp
#include <hip/hip_runtime.h>
#include <stdint.h>
#pragma clang diagnostic ignored "-Wint-to-pointer-cast"

typedef unsigned short u16;
typedef unsigned long long u64;
typedef __bf16 bf16x2 __attribute__((ext_vector_type(2)));
typedef __bf16 bf16x8 __attribute__((ext_vector_type(8)));
typedef float f32x2 __attribute__((ext_vector_type(2)));
typedef float f32x4 __attribute__((ext_vector_type(4)));
typedef float f32x16 __attribute__((ext_vector_type(16)));
typedef unsigned u32x2 __attribute__((ext_vector_type(2)));
typedef unsigned u32x4 __attribute__((ext_vector_type(4)));
typedef short s16x4 __attribute__((ext_vector_type(4)));
typedef __attribute__((address_space(1))) u64 gu64;
#define LDSP(T) __attribute__((address_space(3))) T*
#define DI __device__ __forceinline__
#define RLX_AGENT __ATOMIC_RELAXED, __HIP_MEMORY_SCOPE_AGENT

constexpr int S_ = 2048, D_ = 512, F_ = 1792;
constexpr unsigned KF_OFF = 65536;
constexpr unsigned VF_OFF = KF_OFF + S_ * 16 * 2;
constexpr unsigned OF_OFF = VF_OFF + S_ * 16 * 2;
constexpr unsigned HF_OFF = OF_OFF + S_ * D_ * 2;
constexpr unsigned ST_OFF = HF_OFF + S_ * F_ * 2;
constexpr unsigned CS_OFF = ST_OFF + S_ * 8;
constexpr unsigned PAY_END = CS_OFF + 2 * F_ * 4;
constexpr unsigned CTL_OFF = 0;
constexpr unsigned EPOCH_OFF = CTL_OFF;
constexpr unsigned KVF_OFF = CTL_OFF + 4096;
constexpr unsigned HFL_OFF = CTL_OFF + 8192;
constexpr unsigned OFL_OFF = CTL_OFF + 12288;
constexpr unsigned SEEN_OFF = CTL_OFF + 32768;
constexpr unsigned CSF_OFF = CTL_OFF + 40960;
constexpr int LDS_BYTES = 80 * 1024;
constexpr float QSCALE = 0.25f * 1.4426950408889634f;
#define SPINM 40000
constexpr int SPIN_MAX = SPINM;
#define AP0 3
#define AP1 2
#define AP2 1
#define AP3 1
#define GD 0
#define GP 1
#define GPD GP
#define ATT_CHUNKS 8
#define UPX 0
#define GRID 512
#define QPF 1
#define UPF 1
#define DPF 0
#define PREFETCH 0
#define PF_DELAY 2
#define UP_DELAY 5

#define TS_DECL
#define TS(i)
#define TS_DUMP(kid)
#define TS_PARAM
#define TS_ARG

#define TSD(i) TS(i)
#define TSQ(i)
#define TSA(i) TS(i)
DI f32x16 mfma(u32x4 a, u32x4 b, f32x16 c) {
  return __builtin_amdgcn_mfma_f32_32x32x16_bf16(__builtin_bit_cast(bf16x8, a), __builtin_bit_cast(bf16x8, b), c, 0, 0, 0);
}
DI unsigned cvtpk(float lo, float hi) { f32x2 v = {lo, hi}; bf16x2 b = __builtin_convertvector(v, bf16x2); return __builtin_bit_cast(unsigned, b); }
DI float bfr(float f) { return __uint_as_float(cvtpk(f, f) << 16); }
DI float dot2(unsigned a, unsigned b, float c) { return __builtin_amdgcn_fdot2_f32_bf16(__builtin_bit_cast(bf16x2, a), __builtin_bit_cast(bf16x2, b), c, false); }
DI u32x4 lds128(unsigned addr) { return *(LDSP(const u32x4))(addr); }
DI f32x4 lds128f(unsigned addr) { return *(LDSP(const f32x4))(addr); }
DI f32x2 lds64f(unsigned addr) { return *(LDSP(const f32x2))(addr); }
DI float lds32f(unsigned addr) { return *(LDSP(const float))(addr); }
DI void sts128(unsigned addr, u32x4 v) { *(LDSP(u32x4))(addr) = v; }
DI void sts128f(unsigned addr, f32x4 v) { *(LDSP(f32x4))(addr) = v; }
DI void sts64f(unsigned addr, f32x2 v) { *(LDSP(f32x2))(addr) = v; }
DI void sts32f(unsigned addr, float v) { *(LDSP(float))(addr) = v; }
DI u32x2 ldstr(unsigned addr) { return __builtin_bit_cast(u32x2, __builtin_amdgcn_ds_read_tr16_b64_v4i16((LDSP(s16x4))(addr))); }
DI u32x4 ldstr2(unsigned a0, unsigned a1) { u32x2 lo = ldstr(a0), hi = ldstr(a1); return (u32x4){lo[0], lo[1], hi[0], hi[1]}; }
typedef __amdgpu_buffer_rsrc_t rsrc_t;
DI rsrc_t mkrs(const void* p, unsigned bytes) { return __builtin_amdgcn_make_buffer_rsrc((void*)p, 0, (int)bytes, 0x00020000); }
#define BDMA_(MOD, rs, voff, ldsw, lconst, gconst) do { unsigned t_; \
  asm volatile("s_add_u32 m0, %2, %4\n\ts_mov_b32 %0, %5\n\ts_nop 2\n\tbuffer_load_dwordx4 %1, %3, %0 offen " MOD "lds" \
               : "=&s"(t_) : "v"(voff), "s"(ldsw), "s"(rs), "i"(lconst), "i"(gconst) : "memory", "scc"); } while (0)
#define BDMA(rs, voff, ldsw, lconst, gconst) BDMA_("", rs, voff, ldsw, lconst, gconst)
#define BDMA_SC1(rs, voff, ldsw, lconst, gconst) BDMA_("sc1 ", rs, voff, ldsw, lconst, gconst)
DI void bdma_rt_sc1(rsrc_t rs, unsigned voff, unsigned lds, unsigned goff) {
  lds = __builtin_amdgcn_readfirstlane(lds); goff = __builtin_amdgcn_readfirstlane(goff);
  asm volatile("s_mov_b32 m0, %1\n\ts_nop 3\n\tbuffer_load_dwordx4 %0, %2, %3 offen sc1 lds" :: "v"(voff), "s"(lds), "s"(rs), "s"(goff) : "memory");
}
DI void bdma_rt(rsrc_t rs, unsigned voff, unsigned lds, unsigned goff) {
  lds = __builtin_amdgcn_readfirstlane(lds); goff = __builtin_amdgcn_readfirstlane(goff);
  asm volatile("s_mov_b32 m0, %1\n\ts_nop 3\n\tbuffer_load_dwordx4 %0, %2, %3 offen lds" :: "v"(voff), "s"(lds), "s"(rs), "s"(goff) : "memory");
}
DI unsigned pf_line(rsrc_t rs, unsigned voff) { unsigned d; asm volatile("s_nop 4\n\tbuffer_load_dword %0, %1, %2, 0 offen" : "=v"(d) : "v"(voff), "s"(rs) : "memory"); return d; }
DI unsigned pf_line_sc1(rsrc_t rs, unsigned voff) { unsigned d; asm volatile("s_nop 4\n\tbuffer_load_dword %0, %1, %2, 0 offen sc1" : "=v"(d) : "v"(voff), "s"(rs) : "memory"); return d; }
#define KEEP(x) asm volatile("" :: "v"(x))
constexpr unsigned OOB = 0x7f000000u;
#define SBAR() __builtin_amdgcn_sched_barrier(0)
#define PIN(x) asm volatile("" : "+v"(x))
#define BAR() asm volatile("s_waitcnt lgkmcnt(0)\n\ts_barrier" ::: "memory")
#define DRAIN() asm volatile("s_waitcnt vmcnt(0)" ::: "memory")
template <int N> DI void waitv_bar() { asm volatile("s_waitcnt vmcnt(%0) lgkmcnt(0)\n\ts_barrier" :: "i"(N) : "memory"); }
template <int N> DI void waitv_bar_nl() { asm volatile("s_waitcnt vmcnt(%0)\n\ts_barrier" :: "i"(N) : "memory"); }
template <int C> DI float dppadd(float v) { return v + __uint_as_float(__builtin_amdgcn_update_dpp(0, __float_as_uint(v), C, 0xf, 0xf, true)); }
DI f32x16 zero16() { f32x16 z; for (int i = 0; i < 16; ++i) z[i] = 0.f; return z; }
DI float xhalf_add(float v) {
  auto r = __builtin_amdgcn_permlane32_swap(__float_as_uint(v), __float_as_uint(v), false, false);
  return __uint_as_float(r[0]) + __uint_as_float(r[1]);
}
DI float xhalf_max(float v) {
  auto r = __builtin_amdgcn_permlane32_swap(__float_as_uint(v), __float_as_uint(v), false, false);
  return fmaxf(__uint_as_float(r[0]), __uint_as_float(r[1]));
}
constexpr unsigned ONES2 = 0x3f803f80u;
DI float sum8(u32x4 v, float s) { s = dot2(v[0], ONES2, s); s = dot2(v[1], ONES2, s); s = dot2(v[2], ONES2, s); return dot2(v[3], ONES2, s); }
DI float sq8(u32x4 v, float s) { s = dot2(v[0], v[0], s); s = dot2(v[1], v[1], s); s = dot2(v[2], v[2], s); return dot2(v[3], v[3], s); }
DI u32x4 pack8f(f32x4 a, f32x4 b) { return (u32x4){cvtpk(a[0], a[1]), cvtpk(a[2], a[3]), cvtpk(b[0], b[1]), cvtpk(b[2], b[3])}; }
DI f32x4 q4(const f32x16& a, int g) { return (f32x4){a[4 * g], a[4 * g + 1], a[4 * g + 2], a[4 * g + 3]}; }
DI void st_sc1(char* ws, unsigned off, u32x4 v) {
  __amdgpu_buffer_rsrc_t rs = __builtin_amdgcn_make_buffer_rsrc(ws, 0, (int)PAY_END, 0x00020000);
  __builtin_amdgcn_raw_buffer_store_b128(v, rs, (int)off, 0, 16);
}
DI u64 load_tag(const char* ws) { u64 e; asm volatile("s_load_dwordx2 %0, %1, 0x0\n\ts_waitcnt lgkmcnt(0)" : "=s"(e) : "s"(ws) : "memory"); return e + 1; }
DI void set_tag(char* ws, unsigned off, u64 tag) { __hip_atomic_store((gu64*)(ws + off), tag, RLX_AGENT); }
DI void poll_tags(char* ws, unsigned off, bool active, u64 tag) {
#pragma unroll 1
  for (int it = 0; it < SPIN_MAX; ++it) {
    u64 v = tag;
    if (active) v = __hip_atomic_load((gu64*)(ws + off), RLX_AGENT);
    if (__all(v == tag)) break;
    __builtin_amdgcn_s_sleep(1);
  }
}

DI f32x2 ln_scalars(float s, float ss) {
  float m = s * (1.f / D_);
  float var = fmaxf(ss * (1.f / D_) - m * m, 0.f);
  float sd = bfr(__builtin_amdgcn_sqrtf(bfr(bfr(var) + 1e-5f)));
  return (f32x2){bfr(m), __builtin_amdgcn_rcpf(sd)};
}

DI u32x4 role_q(const u16* x, const u16* wq, const char* ws, u64& tag, int rb, int cb, int w, int lane TS_PARAM) {
  constexpr int NS = 5, SLOT = 16384;
  const int mt = w & 1, nt = (w >> 1) & 1, kh = w >> 2;
  const int hh = lane >> 5;
  const int drow = 8 * w + (lane >> 3), dpc = lane & 7;
  const unsigned xoff = (rb * 64 + drow) * 1024 + ((dpc ^ ((drow >> 1) & 7)) << 4);
  const unsigned woff = drow * 1024 + cb * 128 + ((dpc ^ (((drow >> 1) & 1) << 2)) << 4);
  const rsrc_t rx = mkrs(x, S_ * D_ * 2), rw = mkrs(wq, D_ * D_ * 2);
  const unsigned lw = w * 1024;
#pragma unroll
  for (int j = 0; j < NS; ++j) {
    const unsigned k = (j + cb) & 7;
    bdma_rt(rx, xoff, lw + j * SLOT, k * 128); bdma_rt(rw, woff, lw + j * SLOT + 8192, k * 65536);
  }
  u32x4 lx[3], lwq[3];
#pragma unroll
  for (int j = 0; j < 3; ++j) {
    const unsigned k = (5 + j + cb) & 7;
    lx[j] = __builtin_amdgcn_raw_buffer_load_b128(rx, (int)xoff, k * 128, 0);
    lwq[j] = __builtin_amdgcn_raw_buffer_load_b128(rw, (int)woff, k * 65536, 0);
  }
  const int xr = 32 * mt + (lane & 31), sw = (xr >> 1) & 7;
  const unsigned xa0 = xr * 128 + (((2 * (kh + 2 * mt) + hh) ^ sw) << 4);
  const unsigned xa1 = xr * 128 + (((2 * (kh + 2 - 2 * mt) + hh) ^ sw) << 4);
  const int r4 = (lane & 15) >> 2, p4 = lane & 3, grp = (lane >> 4) & 1;
  const unsigned wa = 8192 + (8 * hh + r4) * 128 + ((nt ^ (r4 >> 1)) << 6) + grp * 32 + p4 * 8 + kh * 2048 + mt * 4096, wb = wa ^ 4096;
  const unsigned sa = w * 1024 + lane * 16;
  f32x16 acc = zero16();
  float s1 = 0.f, s2 = 0.f, cs = 0.f;
  u32x4 fw0[2], fw1[2], fx0[2], fx1[2], fxs[2];
#define Q_RD(i, sb) do { fw0[i] = ldstr2((sb) + wa, (sb) + wa + 512); fx0[i] = lds128((sb) + xa0); fw1[i] = ldstr2((sb) + wb, (sb) + wb + 512); \
    fx1[i] = lds128((sb) + xa1); fxs[i] = lds128((sb) + sa); SBAR(); } while (0)
#define Q_MM(i) do { acc = mfma(fw0[i], fx0[i], acc); cs = sum8(fw0[i], cs); acc = mfma(fw1[i], fx1[i], acc); s1 = sum8(fxs[i], s1); s2 = sq8(fxs[i], s2); \
    PIN(cs); PIN(s1); PIN(s2); PIN(acc); SBAR(); } while (0)
  tag = load_tag(ws);
  TSQ(3);
  waitv_bar<10>();
  TSQ(4);
  Q_RD(0, 0); Q_RD(1, SLOT); Q_MM(0); Q_RD(0, 2 * SLOT); Q_MM(1); Q_MM(0);
  waitv_bar<6>();
  TSQ(5);
  Q_RD(0, 3 * SLOT); Q_RD(1, 4 * SLOT);
  Q_MM(0);
#pragma unroll
  for (int j = 0; j < 3; ++j) { sts128(j * SLOT + sa, lx[j]); sts128(j * SLOT + 8192 + sa, lwq[j]); }
  SBAR();
  Q_MM(1);
  BAR();
  TSQ(6);
  Q_RD(0, 0); Q_RD(1, SLOT); Q_MM(0); Q_RD(0, 2 * SLOT); Q_MM(1); Q_MM(0);
  TSQ(7);
  s1 = dppadd<0xB1>(s1); s2 = dppadd<0xB1>(s2);
  s1 = dppadd<0x4E>(s1); s2 = dppadd<0x4E>(s2);
  s1 = dppadd<0x141>(s1); s2 = dppadd<0x141>(s2);
  cs = xhalf_add(cs);
  const f32x2 st = ln_scalars(s1, s2);
  constexpr unsigned SC = 3 * SLOT, ST = 4 * SLOT;
  if ((lane & 7) == 0) sts64f(ST + lw + (lane >> 3) * 8, st);
  if (lane < 32) sts32f(ST + 8192 + lw + lane * 4, cs);
  f32x4 k0, k1;
  if (kh) { sts128f(SC + sa, q4(acc, 0)); sts128f(SC + 8192 + sa, q4(acc, 1)); k0 = q4(acc, 2); k1 = q4(acc, 3); }
  else    { sts128f(SC + sa, q4(acc, 2)); sts128f(SC + 8192 + sa, q4(acc, 3)); k0 = q4(acc, 0); k1 = q4(acc, 1); }
  BAR();
  const unsigned pp = SC + (sa ^ 4096);
  k0 += lds128f(pp); k1 += lds128f(pp + 8192);
  const unsigned ca = ST + 8192 + nt * 2048 + (kh * 16 + 4 * hh) * 4;
  const f32x4 c0 = (lds128f(ca) + lds128f(ca + 1024)) + (lds128f(ca + 4096) + lds128f(ca + 5120));
  const f32x4 c1 = (lds128f(ca + 32) + lds128f(ca + 1024 + 32)) + (lds128f(ca + 4096 + 32) + lds128f(ca + 5120 + 32));
  const f32x2 rs = lds64f(ST + (xr >> 3) * 1024 + (xr & 7) * 8);
  const float rq = rs[1] * QSCALE;
  return pack8f((k0 - rs[0] * c0) * rq, (k1 - rs[0] * c1) * rq);
}

template <int WHICH> DI void kv_loop(f32x16& acc, float& cs, float& s1, float& s2, unsigned xa0, unsigned xa1, unsigned wa, unsigned sa) {
#pragma unroll
  for (int J = 0; J < 4; ++J) {
    if (J == 0) waitv_bar<6>(); else if (J == 1) waitv_bar<4>(); else if (J == 2) waitv_bar<2>(); else waitv_bar<0>();
    const unsigned sb = J * 16384;
    u32x4 w0 = ldstr2(sb + wa, sb + wa + 128), x0 = lds128(sb + xa0);
    u32x4 w1 = ldstr2(sb + wa + 2048, sb + wa + 2048 + 128), x1 = lds128(sb + xa1);
    u32x4 xs = lds128(sb + sa);
    SBAR();
    acc = WHICH ? mfma(x0, w0, acc) : mfma(w0, x0, acc); cs = sum8(w0, cs); PIN(cs); SBAR();
    acc = WHICH ? mfma(x1, w1, acc) : mfma(w1, x1, acc); cs = sum8(w1, cs); s1 = sum8(xs, s1); s2 = sq8(xs, s2); PIN(cs); PIN(s1); PIN(s2); SBAR();
  }
}
DI void role_kv(const u16* x, const u16* wk, const u16* wv, char* ws, u64& tag, int gmt, int w, int lane TS_PARAM) {
  const int which = w & 1, kq = w >> 1, hh = lane >> 5;
  const int drow = 4 * w + (lane >> 4), dpc = lane & 15;
  const unsigned xoff = (gmt * 32 + drow) * 1024 + ((dpc ^ (drow & 15)) << 4);
  const rsrc_t rx = mkrs(x, S_ * D_ * 2), rw = mkrs(w < 4 ? wk : wv, D_ * 16 * 2);
  const unsigned woff = (w & 3) * 1024 + lane * 16, lw = w * 1024;
#pragma unroll
  for (int J = 0; J < 4; ++J) { const unsigned k = (J + gmt) & 3; bdma_rt(rx, xoff, lw + J * 16384, k * 256); bdma_rt(rw, woff, lw + J * 16384 + 8192, k * 4096); }
  const int xr = lane & 31;
  const unsigned xa0 = xr * 256 + (((2 * kq + hh) ^ (xr & 15)) << 4);
  const unsigned xa1 = xr * 256 + (((2 * (kq + 4) + hh) ^ (xr & 15)) << 4);
  const int r4 = (lane & 15) >> 2, p4 = lane & 3, grp = (lane >> 4) & 1;
  const unsigned wa = 8192 + (16 * kq + 8 * hh + r4) * 32 + grp * 4096 + p4 * 8;
  const unsigned sa = w * 1024 + lane * 16;
  f32x16 acc = zero16();
  float s1 = 0.f, s2 = 0.f, cs = 0.f;
  tag = load_tag(ws);
  TS(1);
  if (which) kv_loop<1>(acc, cs, s1, s2, xa0, xa1, wa, sa); else kv_loop<0>(acc, cs, s1, s2, xa0, xa1, wa, sa);
  TS(2);
  s1 = dppadd<0xB1>(s1); s2 = dppadd<0xB1>(s2);
  s1 = dppadd<0x4E>(s1); s2 = dppadd<0x4E>(s2);
  s1 = dppadd<0x141>(s1); s2 = dppadd<0x141>(s2);
  s1 = dppadd<0x140>(s1); s2 = dppadd<0x140>(s2);
  cs = xhalf_add(cs);
  const f32x2 st = ln_scalars(s1, s2);
  constexpr unsigned STB = 65536, CSB = 65536 + 512;
  if ((lane & 15) == 0) { sts32f(STB + drow * 4, st[0]); sts32f(STB + 128 + drow * 4, st[1]); }
  if (which == 0 && lane < 32) sts32f(CSB + (kq * 32 + lane) * 4, cs);
  const unsigned EX = w * 4096 + lane * 16;
  sts128f(EX, q4(acc, 0)); sts128f(EX + 1024, q4(acc, 1));
  if (which) { sts128f(EX + 2048, q4(acc, 2)); sts128f(EX + 3072, q4(acc, 3)); }
  BAR();
  TS(3);
  if (w == 0) {
    f32x4 a0 = {0, 0, 0, 0}, a1 = a0, c0 = a0, c1 = a0;
#pragma unroll
    for (int k = 0; k < 4; ++k) {
      const unsigned e = k * 8192 + lane * 16, c = CSB + (k * 32 + 4 * hh) * 4;
      a0 += lds128f(e); a1 += lds128f(e + 1024); c0 += lds128f(c); c1 += lds128f(c + 32);
    }
    const float m = lds32f(STB + xr * 4), r = lds32f(STB + 128 + xr * 4);
    st_sc1(ws, KF_OFF + gmt * 1024 + lane * 16, pack8f((a0 - m * c0) * r, (a1 - m * c1) * r));
    DRAIN();
  } else if (w == 1 || w == 3) {
    const int s = w >> 1;
    f32x4 a0 = {0, 0, 0, 0}, a1 = a0; float c = 0.f;
#pragma unroll
    for (int k = 0; k < 4; ++k) {
      const unsigned e = k * 8192 + 4096 + s * 2048 + lane * 16;
      a0 += lds128f(e); a1 += lds128f(e + 1024); c += lds32f(CSB + (k * 32 + xr) * 4);
    }
    const unsigned sp = STB + (16 * s + 4 * hh) * 4;
    const f32x4 m0 = lds128f(sp), m1 = lds128f(sp + 32), r0 = lds128f(sp + 128), r1 = lds128f(sp + 160);
    const u32x4 o = pack8f((a0 - m0 * c) * r0, (a1 - m1 * c) * r1);
    if (xr >= 16) st_sc1(ws, VF_OFF + gmt * 1024 + s * 512 + hh * 256 + (lane & 15) * 16, o);
    DRAIN();
  } else if (w == 2) {
    const f32x2 m = lds64f(STB + (lane & 15) * 8), r = lds64f(STB + 128 + (lane & 15) * 8);
    if (lane < 16) st_sc1(ws, ST_OFF + gmt * 256 + lane * 16, __builtin_bit_cast(u32x4, (f32x4){m[0], r[0], m[1], r[1]}));
    DRAIN();
  }
  TS(4);
  BAR();
  TS(5);
  if (w == 0 && lane < 8) set_tag(ws, KVF_OFF + lane * 512 + gmt * 8, tag);
}

DI void role_cs(const u16* wm_, char* ws, u64 tag, int unit, int cb, int w, int lane) {
  const int tid = w * 64 + lane;
  const char* src = (const char*)wm_ + (size_t)(tid >> 4) * (F_ * 2) + cb * 256 + (tid & 15) * 16;
  u32x4 v[16];
#pragma unroll
  for (int i = 0; i < 16; ++i) v[i] = *(const u32x4*)(src + (size_t)i * 32 * (F_ * 2));
  float a[8] = {0, 0, 0, 0, 0, 0, 0, 0};
  unsigned sel_lo = 0x00003f80u;
  asm volatile("" : "+s"(sel_lo));
#pragma unroll
  for (int i = 0; i < 16; ++i)
#pragma unroll
    for (int k = 0; k < 4; ++k) { a[2 * k] = dot2(v[i][k], sel_lo, a[2 * k]); a[2 * k + 1] = dot2(v[i][k], 0x3f800000u, a[2 * k + 1]); }
  sts128f(tid * 32, (f32x4){a[0], a[1], a[2], a[3]}); sts128f(tid * 32 + 16, (f32x4){a[4], a[5], a[6], a[7]});
  BAR();
  if (w == 0) {
    f32x4 t = {0, 0, 0, 0};
#pragma unroll
    for (int i = 0; i < 16; ++i) t += lds128f((2 * i + (lane >> 5)) * 512 + (lane & 31) * 16);
#pragma unroll
    for (int i = 0; i < 4; ++i) t[i] = xhalf_add(t[i]);
    if (lane < 32) st_sc1(ws, CS_OFF + ((unit & 1) * F_ + cb * 128) * 4 + lane * 16, __builtin_bit_cast(u32x4, t));
    DRAIN();
    if (lane == 0) set_tag(ws, CSF_OFF + ((unit & 1) * 14 + cb) * 128, tag);
  }
  BAR();
}

DI void role_up(const u16* x, const u16* wg, const u16* wu, char* ws, u64& tag, bool need_tag, int rb, int cb, int rep, int w, int lane TS_PARAM) {
  constexpr int NS = 3, SLOT = 24576, NST = 16;
  const int wm = w & 1, wn = w >> 1, hh = lane >> 5;
  const int arow = 16 * w + (lane >> 2);
  const unsigned aoff = (rb * 128 + arow) * 1024 + (((lane & 3) ^ ((arow >> 2) & 3)) << 4);
  const int brow = 4 * w + (lane >> 4);
  const unsigned boff = brow * (F_ * 2) + cb * 256 + (((lane & 15) ^ ((brow & 3) << 2)) << 4);
  const rsrc_t rx = mkrs(x, S_ * D_ * 2), rg = mkrs(wg, D_ * F_ * 2), ru = mkrs(wu, D_ * F_ * 2);
  const unsigned lw = w * 1024;
#pragma unroll
  for (int s = 0; s < NS; ++s) { BDMA(rx, aoff, lw, s * SLOT, s * 64); BDMA(rg, boff, lw, s * SLOT + 8192, s * 32 * F_ * 2); BDMA(ru, boff, lw, s * SLOT + 16384, s * 32 * F_ * 2); }
  unsigned pf = 0;
  {
    const int prow = 96 + (rb & 3) * 104 + (w >> 1) * 26 + (lane >> 1);
    pf = pf_line((w & 1) ? ru : rg, lane < 52 ? prow * (F_ * 2) + cb * 256 + (lane & 1) * 128 : OOB);
  }
  const int r4 = (lane & 15) >> 2, p4 = lane & 3, grp = (lane >> 4) & 1;
  const unsigned pa = 8192 + (8 * hh + r4) * 256 + ((wn ^ r4) << 6) + grp * 32 + p4 * 8;
  const int xr = 64 * wm + (lane & 31), sw = (xr >> 2) & 3;
  const unsigned xa0 = xr * 64 + ((hh ^ sw) << 4), xa1 = xr * 64 + (((2 + hh) ^ sw) << 4);
  constexpr unsigned STB = NS * SLOT, CSB = STB + 1024;
  f32x16 ap0 = zero16(), ap1 = zero16(), aq0 = zero16(), aq1 = zero16();
  u32x4 Ax0, Ax1, Ap, Aq, Bx0, Bx1, Bp, Bq;
  if (need_tag) tag = load_tag(ws);
  waitv_bar<6>();
  if (w == 1 && lane == 0) set_tag(ws, SEEN_OFF + blockIdx.x * 8, tag);
  TS(1);
#define UP_RD(X0, X1, P, Q, pp, xx) do { P = ldstr2((pp), (pp) + 1024); X0 = lds128(xx); X1 = lds128((xx) + 2048); Q = ldstr2((pp) + 8192, (pp) + 8192 + 1024); } while (0)
#define UP_DM(rs, vo, lc, so) asm volatile("s_add_u32 m0, %1, %4\n\ts_nop 0\n\tbuffer_load_dwordx4 %0, %2, %3 offen lds" :: "v"(vo), "s"(lw), "s"(rs), "s"(so), "i"(lc) : "memory", "scc")
#define UP_STAGE(sb, sn) do { \
    UP_RD(Bx0, Bx1, Bp, Bq, (sb) + pa + 4096, (sb) + xa1); SBAR(); \
    ap0 = mfma(Ap, Ax0, ap0); ap1 = mfma(Ap, Ax1, ap1); aq0 = mfma(Aq, Ax0, aq0); aq1 = mfma(Aq, Ax1, aq1); SBAR(); \
    waitv_bar<3>();        \
    UP_RD(Ax0, Ax1, Ap, Aq, (sn) + pa, (sn) + xa0); SBAR(); \
    ap0 = mfma(Bp, Bx0, ap0); SBAR(); UP_DM(rx, aoff, (sb), ka); \
    ap1 = mfma(Bp, Bx1, ap1); SBAR(); UP_DM(rg, boff, (sb) + 8192, kw); \
    aq0 = mfma(Bq, Bx0, aq0); SBAR(); UP_DM(ru, boff, (sb) + 16384, kw); \
    aq1 = mfma(Bq, Bx1, aq1); asm volatile("" : "+v"(ap0), "+v"(ap1), "+v"(aq0), "+v"(aq1)); SBAR(); \
    ka += 64; kw += 32 * F_ * 2; } while (0)
  UP_RD(Ax0, Ax1, Ap, Aq, pa, xa0);
  unsigned ka = 3 * 64, kw = 3 * (32 * F_ * 2);
  __builtin_amdgcn_s_setprio(GP);
#pragma unroll 1
  for (int s = 0; s < 5; ++s) { UP_STAGE(0, SLOT); UP_STAGE(SLOT, 2 * SLOT); UP_STAGE(2 * SLOT, 0); }
  __builtin_amdgcn_s_setprio(3);
  if (w == 0) {
    poll_tags(ws, lane < 4 ? KVF_OFF + rep * 512 + (rb * 4 + lane) * 8 : CSF_OFF + ((lane - 4) * 14 + cb) * 128, lane < 6, tag);
    const rsrc_t rws = mkrs(ws, PAY_END);
    BDMA_SC1(rws, rb * 1024 + lane * 16, lw, STB, ST_OFF);
    BDMA_SC1(rws, cb * 512 + (lane >> 5) * (F_ * 4) + (lane & 31) * 16, lw, CSB, CS_OFF);
  }
  UP_RD(Bx0, Bx1, Bp, Bq, pa + 4096, xa1);
  ap0 = mfma(Ap, Ax0, ap0); ap1 = mfma(Ap, Ax1, ap1); aq0 = mfma(Aq, Ax0, aq0); aq1 = mfma(Aq, Ax1, aq1);
  ap0 = mfma(Bp, Bx0, ap0); ap1 = mfma(Bp, Bx1, ap1); aq0 = mfma(Bq, Bx0, aq0); aq1 = mfma(Bq, Bx1, aq1);
  waitv_bar<0>();
  KEEP(pf);
  TS(2);
  __builtin_amdgcn_s_setprio(3);
  constexpr float GA = -2.f * 0.7978845608028654f * 1.4426950408889634f, GB = GA * 0.044715f;
  const unsigned cpa = CSB + (wn * 32 + 4 * hh) * 4;
#pragma unroll
  for (int m = 0; m < 2; ++m) {
    const f32x16& P = m ? ap1 : ap0; const f32x16& Q = m ? aq1 : aq0;
    const f32x2 rs = lds64f(STB + (xr + 32 * m) * 8);
    const float nm = -rs[0], r2 = rs[1] * rs[1], ca = rs[1] * GA, cbb = r2 * rs[1] * GB, ir2 = __builtin_amdgcn_rcpf(r2);
    u32x4 o[2];
#pragma unroll
    for (int g = 0; g < 4; ++g) {
      const f32x4 cg = lds128f(cpa + g * 32), cu = lds128f(cpa + 512 + g * 32);
#pragma unroll
      for (int i = 0; i < 2; ++i) {
        const f32x2 gg = __builtin_elementwise_fma((f32x2){cg[2 * i], cg[2 * i + 1]}, (f32x2){nm, nm}, (f32x2){P[4 * g + 2 * i], P[4 * g + 2 * i + 1]});
        const f32x2 uu = __builtin_elementwise_fma((f32x2){cu[2 * i], cu[2 * i + 1]}, (f32x2){nm, nm}, (f32x2){Q[4 * g + 2 * i], Q[4 * g + 2 * i + 1]});
        const f32x2 z = gg * __builtin_elementwise_fma(gg * gg, (f32x2){cbb, cbb}, (f32x2){ca, ca});
        const f32x2 e = {__builtin_amdgcn_exp2f(z[0]), __builtin_amdgcn_exp2f(z[1])};
        const f32x2 dn = __builtin_elementwise_fma(e, (f32x2){ir2, ir2}, (f32x2){ir2, ir2});
        const f32x2 h = gg * uu * (f32x2){__builtin_amdgcn_rcpf(dn[0]), __builtin_amdgcn_rcpf(dn[1])};
        o[g >> 1][(g & 1) * 2 + i] = cvtpk(h[0], h[1]);
      }
    }
    const unsigned dst = HF_OFF + ((rb * 4 + wm * 2 + m) * 112 + cb * 8 + wn * 2) * 1024 + lane * 16;
    st_sc1(ws, dst, o[0]); st_sc1(ws, dst + 1024, o[1]);
  }
  DRAIN();
  BAR();
  if (w == 0 && lane == 0) set_tag(ws, HFL_OFF + rb * 128 + cb * 8, tag);
  TS(3);
}

DI u32x4 ld_sc1(char* ws, unsigned off) { return __builtin_amdgcn_raw_buffer_load_b128(mkrs(ws, PAY_END), (int)off, 0, 16); }
DI void role_down(const u16* x, const u16* wd, const u16* wo, u16* out, char* ws, u64 tag, int rb, int cb, int w, int lane TS_PARAM) {
  __builtin_amdgcn_s_setprio(3);
  const int mt = w >> 2, kq = w & 3, hh = lane >> 5;
  const rsrc_t rws = mkrs(ws, PAY_END), rd = mkrs(wd, F_ * D_ * 2), ro = mkrs(wo, D_ * D_ * 2);
  const int r4 = (lane & 15) >> 2, p4 = lane & 3, grp = (lane >> 4) & 1;
  const size_t go = (size_t)(rb * 64 + mt * 32 + (lane & 31)) * D_ + cb * 64 + kq * 16 + hh * 8;
  const u32x4 xr = *(const u32x4*)(x + go);
  unsigned pf0 = 0, pf1 = 0;
  f32x16 a0, a1;
  {
    const unsigned lb = w * 10240;
    const unsigned hoff = (rb * 2 * 112 + w) * 1024 + lane * 16;
    const unsigned boff = (w * 16 + (lane >> 3)) * 1024 + cb * 128 + (((lane & 7) ^ (((lane >> 4) & 1) << 2)) << 4);
    const unsigned wa0 = lb + 2048 + (4 * hh + r4) * 128 + ((r4 >> 1) << 6) + grp * 32 + p4 * 8, wa1 = wa0 ^ 64;
    const unsigned xa = lb + lane * 16;
    const unsigned foff = HFL_OFF + (rb >> 1) * 128 + (lane & 15) * 8;
    unsigned have = 0;
    auto issue = [&](int i, unsigned slot) {
      unsigned ho = OOB;
      if (i < 14) {
        if (!((have >> i) & 1)) {
          __builtin_amdgcn_s_setprio(3);
#pragma unroll 1
          for (int it = 0; it < SPIN_MAX; ++it) {
            const u64 v = __hip_atomic_load((gu64*)(ws + foff), RLX_AGENT);
            have = (unsigned)__ballot(v == tag);
            if ((have >> i) & 1) break;
            __builtin_amdgcn_s_sleep(6);
          }
          __builtin_amdgcn_s_setprio(GD);
        }
        ho = HF_OFF + i * 8192;
      }
      bdma_rt_sc1(rws, hoff, slot, ho);
      bdma_rt_sc1(rws, hoff, slot + 1024, ho + 112 * 1024);
      bdma_rt(rd, boff, slot + 2048, i * 131072);
      bdma_rt(rd, boff, slot + 3072, i * 131072 + 8192);
    };
    f32x16 c00 = zero16(), c01 = zero16(), c10 = zero16(), c11 = zero16();
#define DN_STEP(i, so) do { \
      asm volatile("s_waitcnt vmcnt(4)" ::: "memory"); \
      const u32x4 x0 = lds128(xa + (so)), x1 = lds128(xa + (so) + 1024), u0 = ldstr2(wa0 + (so), wa0 + (so) + 1024), u1 = ldstr2(wa1 + (so), wa1 + (so) + 1024); \
      asm volatile("s_waitcnt lgkmcnt(0)" ::: "memory"); SBAR(); \
      issue((i) + 2, lb + (so)); SBAR(); \
      c00 = mfma(u0, x0, c00); c01 = mfma(u1, x0, c01); c10 = mfma(u0, x1, c10); c11 = mfma(u1, x1, c11); \
      asm volatile("" : "+v"(c00), "+v"(c01), "+v"(c10), "+v"(c11)); SBAR(); } while (0)
    asm volatile("" : "+v"(c00), "+v"(c01), "+v"(c10), "+v"(c11));
    issue(0, lb); issue(1, lb + 4096);
    __builtin_amdgcn_s_setprio(GD);
    TSD(4);
#pragma unroll 1
    for (int i = 0; i < 14; i += 2) { DN_STEP(i, 0); DN_STEP(i + 1, 4096); }
    TSD(5);
    const unsigned eb = w * 8192 + lane * 16, rbs = eb ^ 32768;
    waitv_bar<0>();
    KEEP(pf0); KEEP(pf1);
    if (mt) {
#pragma unroll
      for (int g = 0; g < 4; ++g) { sts128f(eb + g * 1024, q4(c00, g)); sts128f(eb + 4096 + g * 1024, q4(c01, g)); }
      a0 = c10; a1 = c11;
    } else {
#pragma unroll
      for (int g = 0; g < 4; ++g) { sts128f(eb + g * 1024, q4(c10, g)); sts128f(eb + 4096 + g * 1024, q4(c11, g)); }
      a0 = c00; a1 = c01;
    }
    BAR();
    __builtin_amdgcn_s_setprio(3);
#pragma unroll
    for (int g = 0; g < 4; ++g) {
      const f32x4 e0 = lds128f(rbs + g * 1024), e1 = lds128f(rbs + 4096 + g * 1024);
#pragma unroll
      for (int i = 0; i < 4; ++i) { a0[4 * g + i] += e0[i]; a1[4 * g + i] += e1[i]; }
    }
    PIN(a0); PIN(a1);
    BAR();
  }
  const int brow = 8 * w + (lane >> 3);
  const unsigned boff = brow * 1024 + cb * 128 + (((lane & 7) ^ (((brow >> 1) & 1) << 2)) << 4);
  const unsigned lw = w * 1024;
#pragma unroll
  for (int j = 0; j < 8; ++j) BDMA(ro, boff, lw, j * 8192, j * 65536);
  const unsigned wa0 = (16 * kq + 4 * hh + r4) * 128 + ((r4 >> 1) << 6) + grp * 32 + p4 * 8, wa1 = wa0 ^ 64;
  __builtin_amdgcn_s_setprio(GD);
  waitv_bar<0>();
  TSD(6);
  {
    const unsigned ooff = OF_OFF + ((rb * 2 + mt) * 32 + kq) * 1024 + lane * 16;
    const unsigned foff = OFL_OFF + rb * 512 + w * 64 + (lane & 7) * 8;
    u32x4 of[8];
#pragma unroll
    for (int j = 0; j < 8; ++j) of[j] = (u32x4){0, 0, 0, 0};
    unsigned pending = 0xff, loaded = 0;
#pragma unroll 1
    for (int it = 0; it < SPIN_MAX && (pending | loaded); ++it) {
      unsigned rdy = 0;
      if (pending) {
        const u64 v = __hip_atomic_load((gu64*)(ws + foff), RLX_AGENT);
        __builtin_amdgcn_s_setprio(3);
        rdy = (unsigned)__ballot(v == tag) & pending;
        __builtin_amdgcn_s_setprio(GD);
      }
#pragma unroll
      for (int j = 0; j < 8; ++j)
        if (loaded & (1u << j)) {
          a0 = mfma(ldstr2(j * 8192 + wa0, j * 8192 + wa0 + 1024), of[j], a0);
          a1 = mfma(ldstr2(j * 8192 + wa1, j * 8192 + wa1 + 1024), of[j], a1);
        }
#pragma unroll
      for (int j = 0; j < 8; ++j)
        if (rdy & (1u << j)) of[j] = ld_sc1(ws, ooff + j * 4096);
      pending &= ~rdy; loaded = rdy;
      if (pending == 0) __builtin_amdgcn_s_setprio(3);
      if (pending == 0xff) __builtin_amdgcn_s_sleep(8); else if (!rdy && pending) __builtin_amdgcn_s_sleep(2);
    }
  }
  BAR();
  f32x4 k0, k1;
  {
    const unsigned eb = mt * 32768 + kq * 2048 + lane * 16;
    if (kq != 0) { sts128f(eb, q4(a0, 0)); sts128f(eb + 1024, q4(a0, 1)); }
    if (kq != 1) { sts128f(eb + 8192, q4(a0, 2)); sts128f(eb + 8192 + 1024, q4(a0, 3)); }
    if (kq != 2) { sts128f(eb + 16384, q4(a1, 0)); sts128f(eb + 16384 + 1024, q4(a1, 1)); }
    if (kq != 3) { sts128f(eb + 24576, q4(a1, 2)); sts128f(eb + 24576 + 1024, q4(a1, 3)); }
    if (kq == 0) { k0 = q4(a0, 0); k1 = q4(a0, 1); } else if (kq == 1) { k0 = q4(a0, 2); k1 = q4(a0, 3); }
    else if (kq == 2) { k0 = q4(a1, 0); k1 = q4(a1, 1); } else { k0 = q4(a1, 2); k1 = q4(a1, 3); }
  }
  BAR();
#pragma unroll
  for (int i = 1; i < 4; ++i) {
    const unsigned rbs = mt * 32768 + kq * 8192 + ((kq + i) & 3) * 2048 + lane * 16;
    k0 += lds128f(rbs); k1 += lds128f(rbs + 1024);
  }
#pragma unroll
  for (int i = 0; i < 4; ++i) {
    auto r = __builtin_amdgcn_permlane32_swap(__float_as_uint(k0[i]), __float_as_uint(k1[i]), false, false);
    k0[i] = __uint_as_float(r[0]); k1[i] = __uint_as_float(r[1]);
  }
  k0 += (f32x4){__uint_as_float(xr[0] << 16), __uint_as_float(xr[0] & 0xffff0000u), __uint_as_float(xr[1] << 16), __uint_as_float(xr[1] & 0xffff0000u)};
  k1 += (f32x4){__uint_as_float(xr[2] << 16), __uint_as_float(xr[2] & 0xffff0000u), __uint_as_float(xr[3] << 16), __uint_as_float(xr[3] & 0xffff0000u)};
  *(u32x4*)(out + go) = pack8f(k0, k1);
  TS(7);
}

DI u32x4 pack8(const f32x16& p, int b) {
  return (u32x4){cvtpk(p[b], p[b + 1]), cvtpk(p[b + 2], p[b + 3]), cvtpk(p[b + 4], p[b + 5]), cvtpk(p[b + 6], p[b + 7])};
}
DI void role_attn(char* ws, u64 tag, const u32x4 qf, int rb, int cb, int rep, int w, int lane TS_PARAM) {
  constexpr int NS = 4, SLOT = 16640, VT = 1056;
  const int gmt = rb * 2 + (w & 1), head = cb * 4 + ((w >> 1) & 1) * 2 + (w >> 2);
  const int hh = lane >> 5, d = lane & 31;
  const unsigned loff = lane * 16;
  const rsrc_t rws = mkrs(ws, PAY_END);
  auto issue = [&](int c) {
    const unsigned slot = (c % NS) * SLOT;
    bdma_rt_sc1(rws, loff, slot + w * 1024, KF_OFF + c * 8192 + w * 1024);
    bdma_rt_sc1(rws, loff, slot + 8192 + w * VT, VF_OFF + c * 8192 + w * 1024);
  };
  if (w == 0) poll_tags(ws, KVF_OFF + rep * 512 + lane * 8, true, tag);
  BAR();
  TS(2);
  issue(0); issue(1); issue(2);
  if (w == 0) {
    const int t = lane >> 1; const unsigned v = (lane & 1) ? 0u : ONES2;
    sts128((t >> 3) * SLOT + 8192 + (t & 7) * VT + 1024 + (lane & 1) * 16, (u32x4){v, v, v, v});
  }
  const unsigned va0 = 8192 + (d < 16 ? hh * 256 + d * 16 : 1024 + ((d == 16 || d == 20) ? 0 : 16));
  const unsigned va1 = va0 + (d < 16 ? 512 : 0);
  f32x16 o = zero16(), negm, sA, sB;
  waitv_bar<4>();
  u32x4 kf, v0, v1, pl, ph;
  {
    u32x4 k0 = lds128(loff);
    kf = lds128(loff + 1024);
    v0 = lds128(va0);
    v1 = (u32x4){0, 0, 0, 0}; ph = (u32x4){0, 0, 0, 0};
    f32x16 s = mfma(k0, qf, zero16());
    float m = fmaxf(s[0], s[1]);
#pragma unroll
    for (int i = 2; i < 16; i += 2) m = fmaxf(fmaxf(m, s[i]), s[i + 1]);
    m = xhalf_max(m);
#pragma unroll
    for (int i = 0; i < 16; ++i) { negm[i] = -m; sA[i] = s[i] - m; }
  }
  TSA(3);
#define EX(i) C[i] = __builtin_amdgcn_exp2f(C[i])
#define ATILE(CUR, NXT, KN, V1A, V0N) do { f32x16& C = CUR; \
    NXT = mfma(kf, qf, negm); SBAR(); kf = lds128(KN); SBAR(); \
    EX(0); EX(1); EX(2); EX(3); pl[0] = cvtpk(C[0], C[1]); EX(4); EX(5); SBAR(); \
    o = mfma(v1, ph, o); SBAR(); v1 = lds128(V1A); SBAR(); \
    EX(6); pl[1] = cvtpk(C[2], C[3]); EX(7); pl[2] = cvtpk(C[4], C[5]); EX(8); pl[3] = cvtpk(C[6], C[7]); SBAR(); EX(9); EX(10); SBAR(); \
    o = mfma(v0, pl, o); SBAR(); v0 = lds128(V0N); SBAR(); \
    EX(11); EX(12); EX(13); EX(14); EX(15); SBAR(); ph[0] = cvtpk(C[8], C[9]); ph[1] = cvtpk(C[10], C[11]); ph[2] = cvtpk(C[12], C[13]); ph[3] = cvtpk(C[14], C[15]); SBAR(); \
  } while (0)
  for (int c = 0; c < ATT_CHUNKS; ++c) {
    const unsigned sb = (c & 3) * SLOT, sn = ((c + 1) & 3) * SLOT;
    unsigned ka = sb + loff, kn = sn + loff, a0 = sb + va0, a1 = sb + va1, an = sn + va0;
    asm volatile("" : "+v"(ka), "+v"(kn), "+v"(a0), "+v"(a1), "+v"(an));
    __builtin_amdgcn_s_setprio(AP2);
    SBAR();
    ATILE(sA, sB, ka + 2 * 1024, a1 + 0 * VT, a0 + 1 * VT);
    ATILE(sB, sA, ka + 3 * 1024, a1 + 1 * VT, a0 + 2 * VT);
    __builtin_amdgcn_s_setprio(AP3);
    ATILE(sA, sB, ka + 4 * 1024, a1 + 2 * VT, a0 + 3 * VT);
    ATILE(sB, sA, ka + 5 * 1024, a1 + 3 * VT, a0 + 4 * VT);
    if (c < 7) { if (c < 6) waitv_bar_nl<2>(); else waitv_bar_nl<0>(); }
    if (c < 5) issue(c + 3);
    __builtin_amdgcn_s_setprio(AP0);
    SBAR();
    ATILE(sA, sB, ka + 6 * 1024, a1 + 4 * VT, a0 + 5 * VT);
    ATILE(sB, sA, ka + 7 * 1024, a1 + 5 * VT, a0 + 6 * VT);
    __builtin_amdgcn_s_setprio(AP1);
    ATILE(sA, sB, kn, a1 + 6 * VT, a0 + 7 * VT);
    ATILE(sB, sA, kn + 1024, a1 + 7 * VT, an);
  }
  o = mfma(v1, ph, o);
  TSA(4);
  float l = o[8];
  bool bad = !(l <= 1.8446744e19f);
#pragma unroll
  for (int i = 0; i < 8; ++i) bad |= !(__builtin_fabsf(o[i]) <= 3.0e38f);
  if (__any(bad)) {
    o = zero16();
    float m = -__builtin_inff();
    const unsigned cv = (d == 16 || d == 20) ? ONES2 : 0u;
    for (int t = 0; t < 64; ++t) {
      u32x4 kq = ld_sc1(ws, KF_OFF + t * 1024 + loff);
      u32x4 u0 = ld_sc1(ws, VF_OFF + t * 1024 + hh * 256 + (d & 15) * 16), u1 = ld_sc1(ws, VF_OFF + t * 1024 + 512 + hh * 256 + (d & 15) * 16);
      if (d >= 16) { u0 = (u32x4){cv, cv, cv, cv}; u1 = u0; }
      f32x16 s = mfma(kq, qf, zero16());
      float tm = fmaxf(s[0], s[1]);
#pragma unroll
      for (int i = 2; i < 16; i += 2) tm = fmaxf(fmaxf(tm, s[i]), s[i + 1]);
      tm = xhalf_max(tm);
      const float mn = fmaxf(m, tm);
      const float al = __builtin_amdgcn_exp2f(m - mn);
      m = mn;
      f32x16 p;
#pragma unroll
      for (int i = 0; i < 16; ++i) { p[i] = __builtin_amdgcn_exp2f(s[i] - mn); o[i] *= al; }
      o = mfma(u0, pack8(p, 0), o);
      o = mfma(u1, pack8(p, 8), o);
    }
    l = o[8];
  }
  const float inv = __builtin_amdgcn_rcpf(l);
  st_sc1(ws, OF_OFF + (gmt * 32 + head) * 1024 + loff, pack8f(q4(o, 0) * inv, q4(o, 1) * inv));
  DRAIN();
  if (lane == 0) set_tag(ws, OFL_OFF + rb * 512 + ((w & 1) * 4 + (head & 3)) * 64 + cb * 8, tag);
  TSA(5);
}

__global__ void __launch_bounds__(512, 4) k_all(const u16* x, const u16* wq, const u16* wk, const u16* wv, const u16* wo,
                                                const u16* wg, const u16* wu, const u16* wd, u16* out, char* ws) {
  const int lane = threadIdx.x & 63, w = __builtin_amdgcn_readfirstlane(threadIdx.x >> 6);
  const int b = blockIdx.x, xcd = b & 7, j = (b >> 3) & 31;
  TS_DECL; TS(0);
  u64 tag = 0;
  if (b >= 256) {
    __builtin_amdgcn_s_setprio(3);
    const int rb = xcd * 4 + (j >> 3), cb = j & 7;
    const u32x4 qf = role_q(x, wq, ws, tag, rb, cb, w, lane TS_ARG);
    if (w == 1 && lane == 0) set_tag(ws, SEEN_OFF + b * 8, tag);
    TS(1);
    __builtin_amdgcn_s_setprio(2);
    role_attn(ws, tag, qf, rb, cb, xcd, w, lane TS_ARG);
    TS_DUMP(0);
  } else {
    if (j < 8) {
      __builtin_amdgcn_s_setprio(3);
      role_kv(x, wk, wv, ws, tag, xcd * 8 + j, w, lane TS_ARG);
      TS_DUMP(2);
    } else {
      for (int i = 0; i < PF_DELAY; ++i) __builtin_amdgcn_s_sleep(8);
      for (int i = PF_DELAY; i < UP_DELAY; ++i) __builtin_amdgcn_s_sleep(8);
    }
    __builtin_amdgcn_s_setprio(GD);
    if (j >= 4) role_up(x, wg, wu, ws, tag, j >= 8, (xcd >> 1) * 4 + (j & 3), (7 - (j >> 2)) * 2 + (xcd & 1), xcd, w, lane TS_ARG);
    else {
      if (w == 1 && lane == 0) set_tag(ws, SEEN_OFF + b * 8, tag);
      const int unit = (xcd >> 1) * 4 + j;
      if (unit < 14) role_cs((unit & 1) ? wu : wg, ws, tag, unit, (unit >> 1) * 2 + (xcd & 1), w, lane);
      else if (b == 31) {
        poll_tags(ws, SEEN_OFF + (w * 64 + lane) * 8, true, tag);
        BAR();
        if (w == 0 && lane == 0) set_tag(ws, EPOCH_OFF, tag);
      }
    }
    role_down(x, wd, wo, out, ws, tag, xcd * 4 + (j >> 3), j & 7, w, lane TS_ARG);
    TS_DUMP(1);
  }
}

extern "C" void kernel_launch(void* const* d_in, const int* in_sizes, int n_in, void* d_out, int out_size, void* d_ws, size_t ws_size, hipStream_t stream) {
  (void)hipFuncSetAttribute((const void*)k_all, hipFuncAttributeMaxDynamicSharedMemorySize, LDS_BYTES);
  k_all<<<GRID, 512, LDS_BYTES, stream>>>((const u16*)d_in[0], (const u16*)d_in[1], (const u16*)d_in[2], (const u16*)d_in[3], (const u16*)d_in[4],
                                         (const u16*)d_in[6], (const u16*)d_in[7], (const u16*)d_in[8], (u16*)d_out, (char*)d_ws);
}
```

```cpp
#include <hip/hip_runtime.h>
#include <stdint.h>
#pragma clang diagnostic ignored "-Wint-to-pointer-cast"

typedef unsigned short u16;
typedef unsigned long long u64;
typedef __bf16 bf16x2 __attribute__((ext_vector_type(2)));
typedef __bf16 bf16x8 __attribute__((ext_vector_type(8)));
typedef float f32x2 __attribute__((ext_vector_type(2)));
typedef float f32x4 __attribute__((ext_vector_type(4)));
typedef float f32x16 __attribute__((ext_vector_type(16)));
typedef unsigned u32x2 __attribute__((ext_vector_type(2)));
typedef unsigned u32x4 __attribute__((ext_vector_type(4)));
typedef short s16x4 __attribute__((ext_vector_type(4)));
typedef __attribute__((address_space(1))) u64 gu64;
#define LDSP(T) __attribute__((address_space(3))) T*
#define DI __device__ __forceinline__
#define RLX_AGENT __ATOMIC_RELAXED, __HIP_MEMORY_SCOPE_AGENT

constexpr int S_ = 2048, D_ = 512, F_ = 1792;
constexpr unsigned KF_OFF = 65536;
constexpr unsigned VF_OFF = KF_OFF + S_ * 16 * 2;
constexpr unsigned OF_OFF = VF_OFF + S_ * 16 * 2;
constexpr unsigned HF_OFF = OF_OFF + S_ * D_ * 2;
constexpr unsigned ST_OFF = HF_OFF + S_ * F_ * 2;
constexpr unsigned CS_OFF = ST_OFF + S_ * 8;
constexpr unsigned PAY_END = CS_OFF + 2 * F_ * 4;
constexpr unsigned CTL_OFF = 0;
constexpr unsigned EPOCH_OFF = CTL_OFF;
constexpr unsigned KVF_OFF = CTL_OFF + 4096;
constexpr unsigned HFL_OFF = CTL_OFF + 8192;
constexpr unsigned OFL_OFF = CTL_OFF + 12288;
constexpr unsigned SEEN_OFF = CTL_OFF + 32768;
constexpr unsigned CSF_OFF = CTL_OFF + 40960;
constexpr int LDS_BYTES = 80 * 1024;
constexpr float QSCALE = 0.25f * 1.4426950408889634f;
#define SPINM 40000
constexpr int SPIN_MAX = SPINM;
#define AP0 3
#define AP1 2
#define AP2 1
#define AP3 1
#define GD 0
#define GP 0
#define GPD GP
#define ATT_CHUNKS 8
#define UPX 0
#define GRID 512
#define TOUCH 2
#define QPF 1
#define UPF 1
#define DPF 0
#define PREFETCH 0
#define PF_DELAY 2
#define UP_DELAY 5

#define TS_DECL
#define TS(i)
#define TS_DUMP(kid)
#define TS_PARAM
#define TS_ARG

#define TSD(i) TS(i)
#define TSQ(i)
#define TSA(i) TS(i)
DI f32x16 mfma(u32x4 a, u32x4 b, f32x16 c) {
  return __builtin_amdgcn_mfma_f32_32x32x16_bf16(__builtin_bit_cast(bf16x8, a), __builtin_bit_cast(bf16x8, b), c, 0, 0, 0);
}
DI unsigned cvtpk(float lo, float hi) { f32x2 v = {lo, hi}; bf16x2 b = __builtin_convertvector(v, bf16x2); return __builtin_bit_cast(unsigned, b); }
DI float bfr(float f) { return __uint_as_float(cvtpk(f, f) << 16); }
DI float dot2(unsigned a, unsigned b, float c) { return __builtin_amdgcn_fdot2_f32_bf16(__builtin_bit_cast(bf16x2, a), __builtin_bit_cast(bf16x2, b), c, false); }
DI u32x4 lds128(unsigned addr) { return *(LDSP(const u32x4))(addr); }
DI f32x4 lds128f(unsigned addr) { return *(LDSP(const f32x4))(addr); }
DI f32x2 lds64f(unsigned addr) { return *(LDSP(const f32x2))(addr); }
DI float lds32f(unsigned addr) { return *(LDSP(const float))(addr); }
DI void sts128(unsigned addr, u32x4 v) { *(LDSP(u32x4))(addr) = v; }
DI void sts128f(unsigned addr, f32x4 v) { *(LDSP(f32x4))(addr) = v; }
DI void sts64f(unsigned addr, f32x2 v) { *(LDSP(f32x2))(addr) = v; }
DI void sts32f(unsigned addr, float v) { *(LDSP(float))(addr) = v; }
DI u32x2 ldstr(unsigned addr) { return __builtin_bit_cast(u32x2, __builtin_amdgcn_ds_read_tr16_b64_v4i16((LDSP(s16x4))(addr))); }
DI u32x4 ldstr2(unsigned a0, unsigned a1) { u32x2 lo = ldstr(a0), hi = ldstr(a1); return (u32x4){lo[0], lo[1], hi[0], hi[1]}; }
typedef __amdgpu_buffer_rsrc_t rsrc_t;
DI rsrc_t mkrs(const void* p, unsigned bytes) { return __builtin_amdgcn_make_buffer_rsrc((void*)p, 0, (int)bytes, 0x00020000); }
#define BDMA_(MOD, rs, voff, ldsw, lconst, gconst) do { unsigned t_; \
  asm volatile("s_add_u32 m0, %2, %4\n\ts_mov_b32 %0, %5\n\ts_nop 2\n\tbuffer_load_dwordx4 %1, %3, %0 offen " MOD "lds" \
               : "=&s"(t_) : "v"(voff), "s"(ldsw), "s"(rs), "i"(lconst), "i"(gconst) : "memory", "scc"); } while (0)
#define BDMA(rs, voff, ldsw, lconst, gconst) BDMA_("", rs, voff, ldsw, lconst, gconst)
#define BDMA_SC1(rs, voff, ldsw, lconst, gconst) BDMA_("sc1 ", rs, voff, ldsw, lconst, gconst)
DI void bdma_rt_sc1(rsrc_t rs, unsigned voff, unsigned lds, unsigned goff) {
  lds = __builtin_amdgcn_readfirstlane(lds); goff = __builtin_amdgcn_readfirstlane(goff);
  asm volatile("s_mov_b32 m0, %1\n\ts_nop 3\n\tbuffer_load_dwordx4 %0, %2, %3 offen sc1 lds" :: "v"(voff), "s"(lds), "s"(rs), "s"(goff) : "memory");
}
DI void bdma_rt(rsrc_t rs, unsigned voff, unsigned lds, unsigned goff) {
  lds = __builtin_amdgcn_readfirstlane(lds); goff = __builtin_amdgcn_readfirstlane(goff);
  asm volatile("s_mov_b32 m0, %1\n\ts_nop 3\n\tbuffer_load_dwordx4 %0, %2, %3 offen lds" :: "v"(voff), "s"(lds), "s"(rs), "s"(goff) : "memory");
}
DI unsigned pf_line(rsrc_t rs, unsigned voff) { unsigned d; asm volatile("s_nop 4\n\tbuffer_load_dword %0, %1, %2, 0 offen" : "=v"(d) : "v"(voff), "s"(rs) : "memory"); return d; }
DI unsigned pf_line_sc1(rsrc_t rs, unsigned voff) { unsigned d; asm volatile("s_nop 4\n\tbuffer_load_dword %0, %1, %2, 0 offen sc1" : "=v"(d) : "v"(voff), "s"(rs) : "memory"); return d; }
#define KEEP(x) asm volatile("" :: "v"(x))
constexpr unsigned OOB = 0x7f000000u;
#define SBAR() __builtin_amdgcn_sched_barrier(0)
#define PIN(x) asm volatile("" : "+v"(x))
#define BAR() asm volatile("s_waitcnt lgkmcnt(0)\n\ts_barrier" ::: "memory")
#define DRAIN() asm volatile("s_waitcnt vmcnt(0)" ::: "memory")
template <int N> DI void waitv_bar() { asm volatile("s_waitcnt vmcnt(%0) lgkmcnt(0)\n\ts_barrier" :: "i"(N) : "memory"); }
template <int N> DI void waitv_bar_nl() { asm volatile("s_waitcnt vmcnt(%0)\n\ts_barrier" :: "i"(N) : "memory"); }
template <int C> DI float dppadd(float v) { return v + __uint_as_float(__builtin_amdgcn_update_dpp(0, __float_as_uint(v), C, 0xf, 0xf, true)); }
DI f32x16 zero16() { f32x16 z; for (int i = 0; i < 16; ++i) z[i] = 0.f; return z; }
DI float xhalf_add(float v) {
  auto r = __builtin_amdgcn_permlane32_swap(__float_as_uint(v), __float_as_uint(v), false, false);
  return __uint_as_float(r[0]) + __uint_as_float(r[1]);
}
DI float xhalf_max(float v) {
  auto r = __builtin_amdgcn_permlane32_swap(__float_as_uint(v), __float_as_uint(v), false, false);
  return fmaxf(__uint_as_float(r[0]), __uint_as_float(r[1]));
}
constexpr unsigned ONES2 = 0x3f803f80u;
DI float sum8(u32x4 v, float s) { s = dot2(v[0], ONES2, s); s = dot2(v[1], ONES2, s); s = dot2(v[2], ONES2, s); return dot2(v[3], ONES2, s); }
DI float sq8(u32x4 v, float s) { s = dot2(v[0], v[0], s); s = dot2(v[1], v[1], s); s = dot2(v[2], v[2], s); return dot2(v[3], v[3], s); }
DI u32x4 pack8f(f32x4 a, f32x4 b) { return (u32x4){cvtpk(a[0], a[1]), cvtpk(a[2], a[3]), cvtpk(b[0], b[1]), cvtpk(b[2], b[3])}; }
DI f32x4 q4(const f32x16& a, int g) { return (f32x4){a[4 * g], a[4 * g + 1], a[4 * g + 2], a[4 * g + 3]}; }
DI void st_sc1(char* ws, unsigned off, u32x4 v) {
  __amdgpu_buffer_rsrc_t rs = __builtin_amdgcn_make_buffer_rsrc(ws, 0, (int)PAY_END, 0x00020000);
  __builtin_amdgcn_raw_buffer_store_b128(v, rs, (int)off, 0, 16);
}
DI u64 load_tag(const char* ws) { u64 e; asm volatile("s_load_dwordx2 %0, %1, 0x0\n\ts_waitcnt lgkmcnt(0)" : "=s"(e) : "s"(ws) : "memory"); return e + 1; }
DI void set_tag(char* ws, unsigned off, u64 tag) { __hip_atomic_store((gu64*)(ws + off), tag, RLX_AGENT); }
DI void poll_tags(char* ws, unsigned off, bool active, u64 tag) {
#pragma unroll 1
  for (int it = 0; it < SPIN_MAX; ++it) {
    u64 v = tag;
    if (active) v = __hip_atomic_load((gu64*)(ws + off), RLX_AGENT);
    if (__all(v == tag)) break;
    __builtin_amdgcn_s_sleep(1);
  }
}

DI f32x2 ln_scalars(float s, float ss) {
  float m = s * (1.f / D_);
  float var = fmaxf(ss * (1.f / D_) - m * m, 0.f);
  float sd = bfr(__builtin_amdgcn_sqrtf(bfr(bfr(var) + 1e-5f)));
  return (f32x2){bfr(m), __builtin_amdgcn_rcpf(sd)};
}

DI u32x4 role_q(const u16* x, const u16* wq, const char* ws, u64& tag, int rb, int cb, int w, int lane TS_PARAM) {
  constexpr int NS = 5, SLOT = 16384;
  const int mt = w & 1, nt = (w >> 1) & 1, kh = w >> 2;
  const int hh = lane >> 5;
  const int drow = 8 * w + (lane >> 3), dpc = lane & 7;
  const unsigned xoff = (rb * 64 + drow) * 1024 + ((dpc ^ ((drow >> 1) & 7)) << 4);
  const unsigned woff = drow * 1024 + cb * 128 + ((dpc ^ (((drow >> 1) & 1) << 2)) << 4);
  const rsrc_t rx = mkrs(x, S_ * D_ * 2), rw = mkrs(wq, D_ * D_ * 2);
  const unsigned lw = w * 1024;
#pragma unroll
  for (int j = 0; j < NS; ++j) {
    const unsigned k = (j + cb) & 7;
    bdma_rt(rx, xoff, lw + j * SLOT, k * 128); bdma_rt(rw, woff, lw + j * SLOT + 8192, k * 65536);
  }
  u32x4 lx[3], lwq[3];
#pragma unroll
  for (int j = 0; j < 3; ++j) {
    const unsigned k = (5 + j + cb) & 7;
    lx[j] = __builtin_amdgcn_raw_buffer_load_b128(rx, (int)xoff, k * 128, 0);
    lwq[j] = __builtin_amdgcn_raw_buffer_load_b128(rw, (int)woff, k * 65536, 0);
  }
  const int xr = 32 * mt + (lane & 31), sw = (xr >> 1) & 7;
  const unsigned xa0 = xr * 128 + (((2 * (kh + 2 * mt) + hh) ^ sw) << 4);
  const unsigned xa1 = xr * 128 + (((2 * (kh + 2 - 2 * mt) + hh) ^ sw) << 4);
  const int r4 = (lane & 15) >> 2, p4 = lane & 3, grp = (lane >> 4) & 1;
  const unsigned wa = 8192 + (8 * hh + r4) * 128 + ((nt ^ (r4 >> 1)) << 6) + grp * 32 + p4 * 8 + kh * 2048 + mt * 4096, wb = wa ^ 4096;
  const unsigned sa = w * 1024 + lane * 16;
  f32x16 acc = zero16();
  float s1 = 0.f, s2 = 0.f, cs = 0.f;
  u32x4 fw0[2], fw1[2], fx0[2], fx1[2], fxs[2];
#define Q_RD(i, sb) do { fw0[i] = ldstr2((sb) + wa, (sb) + wa + 512); fx0[i] = lds128((sb) + xa0); fw1[i] = ldstr2((sb) + wb, (sb) + wb + 512); \
    fx1[i] = lds128((sb) + xa1); fxs[i] = lds128((sb) + sa); SBAR(); } while (0)
#define Q_MM(i) do { acc = mfma(fw0[i], fx0[i], acc); cs = sum8(fw0[i], cs); acc = mfma(fw1[i], fx1[i], acc); s1 = sum8(fxs[i], s1); s2 = sq8(fxs[i], s2); \
    PIN(cs); PIN(s1); PIN(s2); PIN(acc); SBAR(); } while (0)
  tag = load_tag(ws);
  TSQ(3);
  waitv_bar<10>();
  TSQ(4);
  Q_RD(0, 0); Q_RD(1, SLOT); Q_MM(0); Q_RD(0, 2 * SLOT); Q_MM(1); Q_MM(0);
  waitv_bar<6>();
  TSQ(5);
  Q_RD(0, 3 * SLOT); Q_RD(1, 4 * SLOT);
  Q_MM(0);
#pragma unroll
  for (int j = 0; j < 3; ++j) { sts128(j * SLOT + sa, lx[j]); sts128(j * SLOT + 8192 + sa, lwq[j]); }
  SBAR();
  Q_MM(1);
  BAR();
  TSQ(6);
  Q_RD(0, 0); Q_RD(1, SLOT); Q_MM(0); Q_RD(0, 2 * SLOT); Q_MM(1); Q_MM(0);
  TSQ(7);
  s1 = dppadd<0xB1>(s1); s2 = dppadd<0xB1>(s2);
  s1 = dppadd<0x4E>(s1); s2 = dppadd<0x4E>(s2);
  s1 = dppadd<0x141>(s1); s2 = dppadd<0x141>(s2);
  cs = xhalf_add(cs);
  const f32x2 st = ln_scalars(s1, s2);
  constexpr unsigned SC = 3 * SLOT, ST = 4 * SLOT;
  if ((lane & 7) == 0) sts64f(ST + lw + (lane >> 3) * 8, st);
  if (lane < 32) sts32f(ST + 8192 + lw + lane * 4, cs);
  f32x4 k0, k1;
  if (kh) { sts128f(SC + sa, q4(acc, 0)); sts128f(SC + 8192 + sa, q4(acc, 1)); k0 = q4(acc, 2); k1 = q4(acc, 3); }
  else    { sts128f(SC + sa, q4(acc, 2)); sts128f(SC + 8192 + sa, q4(acc, 3)); k0 = q4(acc, 0); k1 = q4(acc, 1); }
  BAR();
  const unsigned pp = SC + (sa ^ 4096);
  k0 += lds128f(pp); k1 += lds128f(pp + 8192);
  const unsigned ca = ST + 8192 + nt * 2048 + (kh * 16 + 4 * hh) * 4;
  const f32x4 c0 = (lds128f(ca) + lds128f(ca + 1024)) + (lds128f(ca + 4096) + lds128f(ca + 5120));
  const f32x4 c1 = (lds128f(ca + 32) + lds128f(ca + 1024 + 32)) + (lds128f(ca + 4096 + 32) + lds128f(ca + 5120 + 32));
  const f32x2 rs = lds64f(ST + (xr >> 3) * 1024 + (xr & 7) * 8);
  const float rq = rs[1] * QSCALE;
  return pack8f((k0 - rs[0] * c0) * rq, (k1 - rs[0] * c1) * rq);
}

template <int WHICH> DI void kv_loop(f32x16& acc, float& cs, float& s1, float& s2, unsigned xa0, unsigned xa1, unsigned wa, unsigned sa) {
#pragma unroll
  for (int J = 0; J < 4; ++J) {
    if (J == 0) waitv_bar<6>(); else if (J == 1) waitv_bar<4>(); else if (J == 2) waitv_bar<2>(); else waitv_bar<0>();
    const unsigned sb = J * 16384;
    u32x4 w0 = ldstr2(sb + wa, sb + wa + 128), x0 = lds128(sb + xa0);
    u32x4 w1 = ldstr2(sb + wa + 2048, sb + wa + 2048 + 128), x1 = lds128(sb + xa1);
    u32x4 xs = lds128(sb + sa);
    SBAR();
    acc = WHICH ? mfma(x0, w0, acc) : mfma(w0, x0, acc); cs = sum8(w0, cs); PIN(cs); SBAR();
    acc = WHICH ? mfma(x1, w1, acc) : mfma(w1, x1, acc); cs = sum8(w1, cs); s1 = sum8(xs, s1); s2 = sq8(xs, s2); PIN(cs); PIN(s1); PIN(s2); SBAR();
  }
}
DI void role_kv(const u16* x, const u16* wk, const u16* wv, char* ws, u64& tag, int gmt, int w, int lane TS_PARAM) {
  const int which = w & 1, kq = w >> 1, hh = lane >> 5;
  const int drow = 4 * w + (lane >> 4), dpc = lane & 15;
  const unsigned xoff = (gmt * 32 + drow) * 1024 + ((dpc ^ (drow & 15)) << 4);
  const rsrc_t rx = mkrs(x, S_ * D_ * 2), rw = mkrs(w < 4 ? wk : wv, D_ * 16 * 2);
  const unsigned woff = (w & 3) * 1024 + lane * 16, lw = w * 1024;
#pragma unroll
  for (int J = 0; J < 4; ++J) { const unsigned k = (J + gmt) & 3; bdma_rt(rx, xoff, lw + J * 16384, k * 256); bdma_rt(rw, woff, lw + J * 16384 + 8192, k * 4096); }
  const int xr = lane & 31;
  const unsigned xa0 = xr * 256 + (((2 * kq + hh) ^ (xr & 15)) << 4);
  const unsigned xa1 = xr * 256 + (((2 * (kq + 4) + hh) ^ (xr & 15)) << 4);
  const int r4 = (lane & 15) >> 2, p4 = lane & 3, grp = (lane >> 4) & 1;
  const unsigned wa = 8192 + (16 * kq + 8 * hh + r4) * 32 + grp * 4096 + p4 * 8;
  const unsigned sa = w * 1024 + lane * 16;
  f32x16 acc = zero16();
  float s1 = 0.f, s2 = 0.f, cs = 0.f;
  tag = load_tag(ws);
  TS(1);
  if (which) kv_loop<1>(acc, cs, s1, s2, xa0, xa1, wa, sa); else kv_loop<0>(acc, cs, s1, s2, xa0, xa1, wa, sa);
  TS(2);
  s1 = dppadd<0xB1>(s1); s2 = dppadd<0xB1>(s2);
  s1 = dppadd<0x4E>(s1); s2 = dppadd<0x4E>(s2);
  s1 = dppadd<0x141>(s1); s2 = dppadd<0x141>(s2);
  s1 = dppadd<0x140>(s1); s2 = dppadd<0x140>(s2);
  cs = xhalf_add(cs);
  const f32x2 st = ln_scalars(s1, s2);
  constexpr unsigned STB = 65536, CSB = 65536 + 512;
  if ((lane & 15) == 0) { sts32f(STB + drow * 4, st[0]); sts32f(STB + 128 + drow * 4, st[1]); }
  if (which == 0 && lane < 32) sts32f(CSB + (kq * 32 + lane) * 4, cs);
  const unsigned EX = w * 4096 + lane * 16;
  sts128f(EX, q4(acc, 0)); sts128f(EX + 1024, q4(acc, 1));
  if (which) { sts128f(EX + 2048, q4(acc, 2)); sts128f(EX + 3072, q4(acc, 3)); }
  BAR();
  TS(3);
  if (w == 0) {
    f32x4 a0 = {0, 0, 0, 0}, a1 = a0, c0 = a0, c1 = a0;
#pragma unroll
    for (int k = 0; k < 4; ++k) {
      const unsigned e = k * 8192 + lane * 16, c = CSB + (k * 32 + 4 * hh) * 4;
      a0 += lds128f(e); a1 += lds128f(e + 1024); c0 += lds128f(c); c1 += lds128f(c + 32);
    }
    const float m = lds32f(STB + xr * 4), r = lds32f(STB + 128 + xr * 4);
    st_sc1(ws, KF_OFF + gmt * 1024 + lane * 16, pack8f((a0 - m * c0) * r, (a1 - m * c1) * r));
    DRAIN();
  } else if (w == 1 || w == 3) {
    const int s = w >> 1;
    f32x4 a0 = {0, 0, 0, 0}, a1 = a0; float c = 0.f;
#pragma unroll
    for (int k = 0; k < 4; ++k) {
      const unsigned e = k * 8192 + 4096 + s * 2048 + lane * 16;
      a0 += lds128f(e); a1 += lds128f(e + 1024); c += lds32f(CSB + (k * 32 + xr) * 4);
    }
    const unsigned sp = STB + (16 * s + 4 * hh) * 4;
    const f32x4 m0 = lds128f(sp), m1 = lds128f(sp + 32), r0 = lds128f(sp + 128), r1 = lds128f(sp + 160);
    const u32x4 o = pack8f((a0 - m0 * c) * r0, (a1 - m1 * c) * r1);
    if (xr >= 16) st_sc1(ws, VF_OFF + gmt * 1024 + s * 512 + hh * 256 + (lane & 15) * 16, o);
    DRAIN();
  } else if (w == 2) {
    const f32x2 m = lds64f(STB + (lane & 15) * 8), r = lds64f(STB + 128 + (lane & 15) * 8);
    if (lane < 16) st_sc1(ws, ST_OFF + gmt * 256 + lane * 16, __builtin_bit_cast(u32x4, (f32x4){m[0], r[0], m[1], r[1]}));
    DRAIN();
  }
  TS(4);
  BAR();
  TS(5);
  if (w == 0 && lane < 8) set_tag(ws, KVF_OFF + lane * 512 + gmt * 8, tag);
}

DI void role_cs(const u16* wm_, char* ws, u64 tag, int unit, int cb, int w, int lane) {
  const int tid = w * 64 + lane;
  const char* src = (const char*)wm_ + (size_t)(tid >> 4) * (F_ * 2) + cb * 256 + (tid & 15) * 16;
  u32x4 v[16];
#pragma unroll
  for (int i = 0; i < 16; ++i) v[i] = *(const u32x4*)(src + (size_t)i * 32 * (F_ * 2));
  float a[8] = {0, 0, 0, 0, 0, 0, 0, 0};
  unsigned sel_lo = 0x00003f80u;
  asm volatile("" : "+s"(sel_lo));
#pragma unroll
  for (int i = 0; i < 16; ++i)
#pragma unroll
    for (int k = 0; k < 4; ++k) { a[2 * k] = dot2(v[i][k], sel_lo, a[2 * k]); a[2 * k + 1] = dot2(v[i][k], 0x3f800000u, a[2 * k + 1]); }
  sts128f(tid * 32, (f32x4){a[0], a[1], a[2], a[3]}); sts128f(tid * 32 + 16, (f32x4){a[4], a[5], a[6], a[7]});
  BAR();
  if (w == 0) {
    f32x4 t = {0, 0, 0, 0};
#pragma unroll
    for (int i = 0; i < 16; ++i) t += lds128f((2 * i + (lane >> 5)) * 512 + (lane & 31) * 16);
#pragma unroll
    for (int i = 0; i < 4; ++i) t[i] = xhalf_add(t[i]);
    if (lane < 32) st_sc1(ws, CS_OFF + ((unit & 1) * F_ + cb * 128) * 4 + lane * 16, __builtin_bit_cast(u32x4, t));
    DRAIN();
    if (lane == 0) set_tag(ws, CSF_OFF + ((unit & 1) * 14 + cb) * 128, tag);
  }
  BAR();
}

DI void role_up(const u16* x, const u16* wg, const u16* wu, char* ws, u64& tag, bool need_tag, int rb, int cb, int rep, int w, int lane TS_PARAM) {
  constexpr int NS = 3, SLOT = 24576, NST = 16;
  const int wm = w & 1, wn = w >> 1, hh = lane >> 5;
  const int arow = 16 * w + (lane >> 2);
  const unsigned aoff = (rb * 128 + arow) * 1024 + (((lane & 3) ^ ((arow >> 2) & 3)) << 4);
  const int brow = 4 * w + (lane >> 4);
  const unsigned boff = brow * (F_ * 2) + cb * 256 + (((lane & 15) ^ ((brow & 3) << 2)) << 4);
  const rsrc_t rx = mkrs(x, S_ * D_ * 2), rg = mkrs(wg, D_ * F_ * 2), ru = mkrs(wu, D_ * F_ * 2);
  const unsigned lw = w * 1024;
#pragma unroll
  for (int s = 0; s < NS; ++s) { BDMA(rx, aoff, lw, s * SLOT, s * 64); BDMA(rg, boff, lw, s * SLOT + 8192, s * 32 * F_ * 2); BDMA(ru, boff, lw, s * SLOT + 16384, s * 32 * F_ * 2); }
  unsigned pf = 0;
  {
    const int prow = 96 + (rb & 3) * 104 + (w >> 1) * 26 + (lane >> 1);
    pf = pf_line((w & 1) ? ru : rg, lane < 52 ? prow * (F_ * 2) + cb * 256 + (lane & 1) * 128 : OOB);
  }
  const int r4 = (lane & 15) >> 2, p4 = lane & 3, grp = (lane >> 4) & 1;
  const unsigned pa = 8192 + (8 * hh + r4) * 256 + ((wn ^ r4) << 6) + grp * 32 + p4 * 8;
  const int xr = 64 * wm + (lane & 31), sw = (xr >> 2) & 3;
  const unsigned xa0 = xr * 64 + ((hh ^ sw) << 4), xa1 = xr * 64 + (((2 + hh) ^ sw) << 4);
  constexpr unsigned STB = NS * SLOT, CSB = STB + 1024;
  f32x16 ap0 = zero16(), ap1 = zero16(), aq0 = zero16(), aq1 = zero16();
  u32x4 Ax0, Ax1, Ap, Aq, Bx0, Bx1, Bp, Bq;
  if (need_tag) tag = load_tag(ws);
  waitv_bar<6>();
  if (w == 1 && lane == 0) set_tag(ws, SEEN_OFF + blockIdx.x * 8, tag);
  TS(1);
#define UP_RD(X0, X1, P, Q, pp, xx) do { P = ldstr2((pp), (pp) + 1024); X0 = lds128(xx); X1 = lds128((xx) + 2048); Q = ldstr2((pp) + 8192, (pp) + 8192 + 1024); } while (0)
#define UP_DM(rs, vo, lc, so) asm volatile("s_add_u32 m0, %1, %4\n\ts_nop 0\n\tbuffer_load_dwordx4 %0, %2, %3 offen lds" :: "v"(vo), "s"(lw), "s"(rs), "s"(so), "i"(lc) : "memory", "scc")
#define UP_STAGE(sb, sn) do { \
    UP_RD(Bx0, Bx1, Bp, Bq, (sb) + pa + 4096, (sb) + xa1); SBAR(); \
    ap0 = mfma(Ap, Ax0, ap0); ap1 = mfma(Ap, Ax1, ap1); aq0 = mfma(Aq, Ax0, aq0); aq1 = mfma(Aq, Ax1, aq1); SBAR(); \
    waitv_bar<3>();        \
    UP_RD(Ax0, Ax1, Ap, Aq, (sn) + pa, (sn) + xa0); SBAR(); \
    ap0 = mfma(Bp, Bx0, ap0); SBAR(); UP_DM(rx, aoff, (sb), ka); \
    ap1 = mfma(Bp, Bx1, ap1); SBAR(); UP_DM(rg, boff, (sb) + 8192, kw); \
    aq0 = mfma(Bq, Bx0, aq0); SBAR(); UP_DM(ru, boff, (sb) + 16384, kw); \
    aq1 = mfma(Bq, Bx1, aq1); asm volatile("" : "+v"(ap0), "+v"(ap1), "+v"(aq0), "+v"(aq1)); SBAR(); \
    ka += 64; kw += 32 * F_ * 2; } while (0)
  UP_RD(Ax0, Ax1, Ap, Aq, pa, xa0);
  unsigned ka = 3 * 64, kw = 3 * (32 * F_ * 2);
  __builtin_amdgcn_s_setprio(GP);
#pragma unroll 1
  for (int s = 0; s < 5; ++s) { UP_STAGE(0, SLOT); UP_STAGE(SLOT, 2 * SLOT); UP_STAGE(2 * SLOT, 0); }
  __builtin_amdgcn_s_setprio(3);
  if (w == 0) {
    poll_tags(ws, lane < 4 ? KVF_OFF + rep * 512 + (rb * 4 + lane) * 8 : CSF_OFF + ((lane - 4) * 14 + cb) * 128, lane < 6, tag);
    const rsrc_t rws = mkrs(ws, PAY_END);
    BDMA_SC1(rws, rb * 1024 + lane * 16, lw, STB, ST_OFF);
    BDMA_SC1(rws, cb * 512 + (lane >> 5) * (F_ * 4) + (lane & 31) * 16, lw, CSB, CS_OFF);
  }
  UP_RD(Bx0, Bx1, Bp, Bq, pa + 4096, xa1);
  ap0 = mfma(Ap, Ax0, ap0); ap1 = mfma(Ap, Ax1, ap1); aq0 = mfma(Aq, Ax0, aq0); aq1 = mfma(Aq, Ax1, aq1);
  ap0 = mfma(Bp, Bx0, ap0); ap1 = mfma(Bp, Bx1, ap1); aq0 = mfma(Bq, Bx0, aq0); aq1 = mfma(Bq, Bx1, aq1);
  waitv_bar<0>();
  KEEP(pf);
  TS(2);
  __builtin_amdgcn_s_setprio(3);
  constexpr float GA = -2.f * 0.7978845608028654f * 1.4426950408889634f, GB = GA * 0.044715f;
  const unsigned cpa = CSB + (wn * 32 + 4 * hh) * 4;
#pragma unroll
  for (int m = 0; m < 2; ++m) {
    const f32x16& P = m ? ap1 : ap0; const f32x16& Q = m ? aq1 : aq0;
    const f32x2 rs = lds64f(STB + (xr + 32 * m) * 8);
    const float nm = -rs[0], r2 = rs[1] * rs[1], ca = rs[1] * GA, cbb = r2 * rs[1] * GB, ir2 = __builtin_amdgcn_rcpf(r2);
    u32x4 o[2];
#pragma unroll
    for (int g = 0; g < 4; ++g) {
      const f32x4 cg = lds128f(cpa + g * 32), cu = lds128f(cpa + 512 + g * 32);
#pragma unroll
      for (int i = 0; i < 2; ++i) {
        const f32x2 gg = __builtin_elementwise_fma((f32x2){cg[2 * i], cg[2 * i + 1]}, (f32x2){nm, nm}, (f32x2){P[4 * g + 2 * i], P[4 * g + 2 * i + 1]});
        const f32x2 uu = __builtin_elementwise_fma((f32x2){cu[2 * i], cu[2 * i + 1]}, (f32x2){nm, nm}, (f32x2){Q[4 * g + 2 * i], Q[4 * g + 2 * i + 1]});
        const f32x2 z = gg * __builtin_elementwise_fma(gg * gg, (f32x2){cbb, cbb}, (f32x2){ca, ca});
        const f32x2 e = {__builtin_amdgcn_exp2f(z[0]), __builtin_amdgcn_exp2f(z[1])};
        const f32x2 dn = __builtin_elementwise_fma(e, (f32x2){ir2, ir2}, (f32x2){ir2, ir2});
        const f32x2 h = gg * uu * (f32x2){__builtin_amdgcn_rcpf(dn[0]), __builtin_amdgcn_rcpf(dn[1])};
        o[g >> 1][(g & 1) * 2 + i] = cvtpk(h[0], h[1]);
      }
    }
    const unsigned dst = HF_OFF + ((rb * 4 + wm * 2 + m) * 112 + cb * 8 + wn * 2) * 1024 + lane * 16;
    st_sc1(ws, dst, o[0]); st_sc1(ws, dst + 1024, o[1]);
  }
  DRAIN();
  BAR();
  if (w == 0 && lane == 0) set_tag(ws, HFL_OFF + rb * 128 + cb * 8, tag);
  TS(3);
}

DI u32x4 ld_sc1(char* ws, unsigned off) { return __builtin_amdgcn_raw_buffer_load_b128(mkrs(ws, PAY_END), (int)off, 0, 16); }
DI void role_down(const u16* x, const u16* wd, const u16* wo, u16* out, char* ws, u64 tag, int rb, int cb, int w, int lane TS_PARAM) {
  __builtin_amdgcn_s_setprio(3);
  const int mt = w >> 2, kq = w & 3, hh = lane >> 5;
  const rsrc_t rws = mkrs(ws, PAY_END), rd = mkrs(wd, F_ * D_ * 2), ro = mkrs(wo, D_ * D_ * 2);
  const int r4 = (lane & 15) >> 2, p4 = lane & 3, grp = (lane >> 4) & 1;
  const size_t go = (size_t)(rb * 64 + mt * 32 + (lane & 31)) * D_ + cb * 64 + kq * 16 + hh * 8;
  const u32x4 xr = *(const u32x4*)(x + go);
  unsigned pf0 = 0, pf1 = 0;
  f32x16 a0, a1;
  {
    const unsigned lb = w * 10240;
    const unsigned hoff = (rb * 2 * 112 + w) * 1024 + lane * 16;
    const unsigned boff = (w * 16 + (lane >> 3)) * 1024 + cb * 128 + (((lane & 7) ^ (((lane >> 4) & 1) << 2)) << 4);
    const unsigned wa0 = lb + 2048 + (4 * hh + r4) * 128 + ((r4 >> 1) << 6) + grp * 32 + p4 * 8, wa1 = wa0 ^ 64;
    const unsigned xa = lb + lane * 16;
    const unsigned foff = HFL_OFF + (rb >> 1) * 128 + (lane & 15) * 8;
    unsigned have = 0;
    auto issue = [&](int i, unsigned slot) {
      unsigned ho = OOB;
      if (i < 14) {
        if (!((have >> i) & 1)) {
          __builtin_amdgcn_s_setprio(3);
#pragma unroll 1
          for (int it = 0; it < SPIN_MAX; ++it) {
            const u64 v = __hip_atomic_load((gu64*)(ws + foff), RLX_AGENT);
            have = (unsigned)__ballot(v == tag);
            if ((have >> i) & 1) break;
            __builtin_amdgcn_s_sleep(6);
          }
          __builtin_amdgcn_s_setprio(GD);
        }
        ho = HF_OFF + i * 8192;
      }
      bdma_rt_sc1(rws, hoff, slot, ho);
      bdma_rt_sc1(rws, hoff, slot + 1024, ho + 112 * 1024);
      bdma_rt(rd, boff, slot + 2048, i * 131072);
      bdma_rt(rd, boff, slot + 3072, i * 131072 + 8192);
    };
    f32x16 c00 = zero16(), c01 = zero16(), c10 = zero16(), c11 = zero16();
#define DN_STEP(i, so) do { \
      asm volatile("s_waitcnt vmcnt(4)" ::: "memory"); \
      const u32x4 x0 = lds128(xa + (so)), x1 = lds128(xa + (so) + 1024), u0 = ldstr2(wa0 + (so), wa0 + (so) + 1024), u1 = ldstr2(wa1 + (so), wa1 + (so) + 1024); \
      asm volatile("s_waitcnt lgkmcnt(0)" ::: "memory"); SBAR(); \
      issue((i) + 2, lb + (so)); SBAR(); \
      c00 = mfma(u0, x0, c00); c01 = mfma(u1, x0, c01); c10 = mfma(u0, x1, c10); c11 = mfma(u1, x1, c11); \
      asm volatile("" : "+v"(c00), "+v"(c01), "+v"(c10), "+v"(c11)); SBAR(); } while (0)
    asm volatile("" : "+v"(c00), "+v"(c01), "+v"(c10), "+v"(c11));
    issue(0, lb); issue(1, lb + 4096);
    __builtin_amdgcn_s_setprio(GD);
    TSD(4);
#pragma unroll 1
    for (int i = 0; i < 14; i += 2) { DN_STEP(i, 0); DN_STEP(i + 1, 4096); }
    TSD(5);
    const unsigned eb = w * 8192 + lane * 16, rbs = eb ^ 32768;
    waitv_bar<0>();
    KEEP(pf0); KEEP(pf1);
    if (mt) {
#pragma unroll
      for (int g = 0; g < 4; ++g) { sts128f(eb + g * 1024, q4(c00, g)); sts128f(eb + 4096 + g * 1024, q4(c01, g)); }
      a0 = c10; a1 = c11;
    } else {
#pragma unroll
      for (int g = 0; g < 4; ++g) { sts128f(eb + g * 1024, q4(c10, g)); sts128f(eb + 4096 + g * 1024, q4(c11, g)); }
      a0 = c00; a1 = c01;
    }
    BAR();
    __builtin_amdgcn_s_setprio(3);
#pragma unroll
    for (int g = 0; g < 4; ++g) {
      const f32x4 e0 = lds128f(rbs + g * 1024), e1 = lds128f(rbs + 4096 + g * 1024);
#pragma unroll
      for (int i = 0; i < 4; ++i) { a0[4 * g + i] += e0[i]; a1[4 * g + i] += e1[i]; }
    }
    PIN(a0); PIN(a1);
    BAR();
  }
  const int brow = 8 * w + (lane >> 3);
  const unsigned boff = brow * 1024 + cb * 128 + (((lane & 7) ^ (((brow >> 1) & 1) << 2)) << 4);
  const unsigned lw = w * 1024;
#pragma unroll
  for (int j = 0; j < 8; ++j) BDMA(ro, boff, lw, j * 8192, j * 65536);
  const unsigned wa0 = (16 * kq + 4 * hh + r4) * 128 + ((r4 >> 1) << 6) + grp * 32 + p4 * 8, wa1 = wa0 ^ 64;
  __builtin_amdgcn_s_setprio(GD);
  waitv_bar<0>();
  TSD(6);
  {
    const unsigned ooff = OF_OFF + ((rb * 2 + mt) * 32 + kq) * 1024 + lane * 16;
    const unsigned foff = OFL_OFF + rb * 512 + w * 64 + (lane & 7) * 8;
    u32x4 of[8];
#pragma unroll
    for (int j = 0; j < 8; ++j) of[j] = (u32x4){0, 0, 0, 0};
    unsigned pending = 0xff, loaded = 0, landed = 0, next = 0;
#pragma unroll 1
    for (int it = 0; it < SPIN_MAX && next < 8; ++it) {
      unsigned rdy = 0;
      if (pending) {
        const u64 v = __hip_atomic_load((gu64*)(ws + foff), RLX_AGENT);
        __builtin_amdgcn_s_setprio(3);
        rdy = (unsigned)__ballot(v == tag) & pending;
        __builtin_amdgcn_s_setprio(GD);
      }
      landed |= loaded;
#pragma unroll
      for (int j = 0; j < 8; ++j)
        if (next == j && (landed & (1u << j))) {
          a0 = mfma(ldstr2(j * 8192 + wa0, j * 8192 + wa0 + 1024), of[j], a0);
          a1 = mfma(ldstr2(j * 8192 + wa1, j * 8192 + wa1 + 1024), of[j], a1);
          ++next;
        }
#pragma unroll
      for (int j = 0; j < 8; ++j)
        if (rdy & (1u << j)) of[j] = ld_sc1(ws, ooff + j * 4096);
      pending &= ~rdy; loaded = rdy;
      if (pending == 0) __builtin_amdgcn_s_setprio(3);
      if (pending == 0xff) __builtin_amdgcn_s_sleep(8); else if (!rdy && pending) __builtin_amdgcn_s_sleep(2);
    }
  }
  BAR();
  f32x4 k0, k1;
  {
    const unsigned eb = mt * 32768 + kq * 2048 + lane * 16;
    if (kq != 0) { sts128f(eb, q4(a0, 0)); sts128f(eb + 1024, q4(a0, 1)); }
    if (kq != 1) { sts128f(eb + 8192, q4(a0, 2)); sts128f(eb + 8192 + 1024, q4(a0, 3)); }
    if (kq != 2) { sts128f(eb + 16384, q4(a1, 0)); sts128f(eb + 16384 + 1024, q4(a1, 1)); }
    if (kq != 3) { sts128f(eb + 24576, q4(a1, 2)); sts128f(eb + 24576 + 1024, q4(a1, 3)); }
    if (kq == 0) { k0 = q4(a0, 0); k1 = q4(a0, 1); } else if (kq == 1) { k0 = q4(a0, 2); k1 = q4(a0, 3); }
    else if (kq == 2) { k0 = q4(a1, 0); k1 = q4(a1, 1); } else { k0 = q4(a1, 2); k1 = q4(a1, 3); }
  }
  BAR();
#pragma unroll
  for (int i = 1; i < 4; ++i) {
    const unsigned rbs = mt * 32768 + kq * 8192 + ((kq + i) & 3) * 2048 + lane * 16;
    k0 += lds128f(rbs); k1 += lds128f(rbs + 1024);
  }
#pragma unroll
  for (int i = 0; i < 4; ++i) {
    auto r = __builtin_amdgcn_permlane32_swap(__float_as_uint(k0[i]), __float_as_uint(k1[i]), false, false);
    k0[i] = __uint_as_float(r[0]); k1[i] = __uint_as_float(r[1]);
  }
  k0 += (f32x4){__uint_as_float(xr[0] << 16), __uint_as_float(xr[0] & 0xffff0000u), __uint_as_float(xr[1] << 16), __uint_as_float(xr[1] & 0xffff0000u)};
  k1 += (f32x4){__uint_as_float(xr[2] << 16), __uint_as_float(xr[2] & 0xffff0000u), __uint_as_float(xr[3] << 16), __uint_as_float(xr[3] & 0xffff0000u)};
  *(u32x4*)(out + go) = pack8f(k0, k1);
  TS(7);
}

DI u32x4 pack8(const f32x16& p, int b) {
  return (u32x4){cvtpk(p[b], p[b + 1]), cvtpk(p[b + 2], p[b + 3]), cvtpk(p[b + 4], p[b + 5]), cvtpk(p[b + 6], p[b + 7])};
}
DI void role_attn(char* ws, u64 tag, const u32x4 qf, int rb, int cb, int rep, int w, int lane TS_PARAM) {
  constexpr int NS = 4, SLOT = 16640, VT = 1056;
  const int gmt = rb * 2 + (w & 1), head = cb * 4 + ((w >> 1) & 1) * 2 + (w >> 2);
  const int hh = lane >> 5, d = lane & 31;
  const unsigned loff = lane * 16;
  const rsrc_t rws = mkrs(ws, PAY_END);
  auto issue = [&](int c) {
    const unsigned slot = (c % NS) * SLOT;
    bdma_rt_sc1(rws, loff, slot + w * 1024, KF_OFF + c * 8192 + w * 1024);
    bdma_rt_sc1(rws, loff, slot + 8192 + w * VT, VF_OFF + c * 8192 + w * 1024);
  };
  if (w == 0) poll_tags(ws, KVF_OFF + rep * 512 + lane * 8, true, tag);
  BAR();
  TS(2);
  issue(0); issue(1); issue(2);
  if (w == 0) {
    const int t = lane >> 1; const unsigned v = (lane & 1) ? 0u : ONES2;
    sts128((t >> 3) * SLOT + 8192 + (t & 7) * VT + 1024 + (lane & 1) * 16, (u32x4){v, v, v, v});
  }
  const unsigned va0 = 8192 + (d < 16 ? hh * 256 + d * 16 : 1024 + ((d == 16 || d == 20) ? 0 : 16));
  const unsigned va1 = va0 + (d < 16 ? 512 : 0);
  f32x16 o = zero16(), negm, sA, sB;
  waitv_bar<4>();
  u32x4 kf, v0, v1, pl, ph;
  {
    u32x4 k0 = lds128(loff);
    kf = lds128(loff + 1024);
    v0 = lds128(va0);
    v1 = (u32x4){0, 0, 0, 0}; ph = (u32x4){0, 0, 0, 0};
    f32x16 s = mfma(k0, qf, zero16());
    float m = fmaxf(s[0], s[1]);
#pragma unroll
    for (int i = 2; i < 16; i += 2) m = fmaxf(fmaxf(m, s[i]), s[i + 1]);
    m = xhalf_max(m);
#pragma unroll
    for (int i = 0; i < 16; ++i) { negm[i] = -m; sA[i] = s[i] - m; }
  }
  TSA(3);
#define EX(i) C[i] = __builtin_amdgcn_exp2f(C[i])
#define ATILE(CUR, NXT, KN, V1A, V0N) do { f32x16& C = CUR; \
    NXT = mfma(kf, qf, negm); SBAR(); kf = lds128(KN); SBAR(); \
    EX(0); EX(1); EX(2); EX(3); pl[0] = cvtpk(C[0], C[1]); EX(4); EX(5); SBAR(); \
    o = mfma(v1, ph, o); SBAR(); v1 = lds128(V1A); SBAR(); \
    EX(6); pl[1] = cvtpk(C[2], C[3]); EX(7); pl[2] = cvtpk(C[4], C[5]); EX(8); pl[3] = cvtpk(C[6], C[7]); SBAR(); EX(9); EX(10); SBAR(); \
    o = mfma(v0, pl, o); SBAR(); v0 = lds128(V0N); SBAR(); \
    EX(11); EX(12); EX(13); EX(14); EX(15); SBAR(); ph[0] = cvtpk(C[8], C[9]); ph[1] = cvtpk(C[10], C[11]); ph[2] = cvtpk(C[12], C[13]); ph[3] = cvtpk(C[14], C[15]); SBAR(); \
  } while (0)
  for (int c = 0; c < ATT_CHUNKS; ++c) {
    const unsigned sb = (c & 3) * SLOT, sn = ((c + 1) & 3) * SLOT;
    unsigned ka = sb + loff, kn = sn + loff, a0 = sb + va0, a1 = sb + va1, an = sn + va0;
    asm volatile("" : "+v"(ka), "+v"(kn), "+v"(a0), "+v"(a1), "+v"(an));
    __builtin_amdgcn_s_setprio(AP2);
    SBAR();
    ATILE(sA, sB, ka + 2 * 1024, a1 + 0 * VT, a0 + 1 * VT);
    ATILE(sB, sA, ka + 3 * 1024, a1 + 1 * VT, a0 + 2 * VT);
    __builtin_amdgcn_s_setprio(AP3);
    ATILE(sA, sB, ka + 4 * 1024, a1 + 2 * VT, a0 + 3 * VT);
    ATILE(sB, sA, ka + 5 * 1024, a1 + 3 * VT, a0 + 4 * VT);
    if (c < 7) { if (c < 6) waitv_bar_nl<2>(); else waitv_bar_nl<0>(); }
    if (c < 5) issue(c + 3);
    __builtin_amdgcn_s_setprio(AP0);
    SBAR();
    ATILE(sA, sB, ka + 6 * 1024, a1 + 4 * VT, a0 + 5 * VT);
    ATILE(sB, sA, ka + 7 * 1024, a1 + 5 * VT, a0 + 6 * VT);
    __builtin_amdgcn_s_setprio(AP1);
    ATILE(sA, sB, kn, a1 + 6 * VT, a0 + 7 * VT);
    ATILE(sB, sA, kn + 1024, a1 + 7 * VT, an);
  }
  o = mfma(v1, ph, o);
  TSA(4);
  float l = o[8];
  bool bad = !(l <= 1.8446744e19f);
#pragma unroll
  for (int i = 0; i < 8; ++i) bad |= !(__builtin_fabsf(o[i]) <= 3.0e38f);
  if (__any(bad)) {
    o = zero16();
    float m = -__builtin_inff();
    const unsigned cv = (d == 16 || d == 20) ? ONES2 : 0u;
    for (int t = 0; t < 64; ++t) {
      u32x4 kq = ld_sc1(ws, KF_OFF + t * 1024 + loff);
      u32x4 u0 = ld_sc1(ws, VF_OFF + t * 1024 + hh * 256 + (d & 15) * 16), u1 = ld_sc1(ws, VF_OFF + t * 1024 + 512 + hh * 256 + (d & 15) * 16);
      if (d >= 16) { u0 = (u32x4){cv, cv, cv, cv}; u1 = u0; }
      f32x16 s = mfma(kq, qf, zero16());
      float tm = fmaxf(s[0], s[1]);
#pragma unroll
      for (int i = 2; i < 16; i += 2) tm = fmaxf(fmaxf(tm, s[i]), s[i + 1]);
      tm = xhalf_max(tm);
      const float mn = fmaxf(m, tm);
      const float al = __builtin_amdgcn_exp2f(m - mn);
      m = mn;
      f32x16 p;
#pragma unroll
      for (int i = 0; i < 16; ++i) { p[i] = __builtin_amdgcn_exp2f(s[i] - mn); o[i] *= al; }
      o = mfma(u0, pack8(p, 0), o);
      o = mfma(u1, pack8(p, 8), o);
    }
    l = o[8];
  }
  const float inv = __builtin_amdgcn_rcpf(l);
  st_sc1(ws, OF_OFF + (gmt * 32 + head) * 1024 + loff, pack8f(q4(o, 0) * inv, q4(o, 1) * inv));
  DRAIN();
  if (lane == 0) set_tag(ws, OFL_OFF + rb * 512 + ((w & 1) * 4 + (head & 3)) * 64 + cb * 8, tag);
  TSA(5);
}

__global__ void __launch_bounds__(512, 4) k_all(const u16* x, const u16* wq, const u16* wk, const u16* wv, const u16* wo,
                                                const u16* wg, const u16* wu, const u16* wd, u16* out, char* ws) {
  const int lane = threadIdx.x & 63, w = __builtin_amdgcn_readfirstlane(threadIdx.x >> 6);
  const int b = blockIdx.x, xcd = b & 7, j = (b >> 3) & 31;
  TS_DECL; TS(0);
  u64 tag = 0;
  unsigned tch = 0;
  if (w == 6 && j < 4 && b < 256) {
    u64 pc; asm volatile("s_getpc_b64 %0" : "=s"(pc));
    const char* p = (const char*)(pc & ~127ull) + (j * 64 + lane) * 128;
    asm volatile("global_load_dword %0, %1, off" : "=v"(tch) : "v"(p) : "memory");
  }
  if (b >= 256) {
    __builtin_amdgcn_s_setprio(3);
    const int rb = xcd * 4 + (j >> 3), cb = j & 7;
    const u32x4 qf = role_q(x, wq, ws, tag, rb, cb, w, lane TS_ARG);
    if (w == 1 && lane == 0) set_tag(ws, SEEN_OFF + b * 8, tag);
    TS(1);
    __builtin_amdgcn_s_setprio(2);
    role_attn(ws, tag, qf, rb, cb, xcd, w, lane TS_ARG);
    TS_DUMP(0);
    KEEP(tch);
  } else {
    if (j < 8) {
      __builtin_amdgcn_s_setprio(3);
      role_kv(x, wk, wv, ws, tag, xcd * 8 + j, w, lane TS_ARG);
      TS_DUMP(2);
    } else {
      for (int i = 0; i < PF_DELAY; ++i) __builtin_amdgcn_s_sleep(8);
      for (int i = PF_DELAY; i < UP_DELAY; ++i) __builtin_amdgcn_s_sleep(8);
    }
    __builtin_amdgcn_s_setprio(GD);
    if (j >= 4) role_up(x, wg, wu, ws, tag, j >= 8, (xcd >> 1) * 4 + (j & 3), (7 - (j >> 2)) * 2 + (xcd & 1), xcd, w, lane TS_ARG);
    else {
      if (w == 1 && lane == 0) set_tag(ws, SEEN_OFF + b * 8, tag);
      const int unit = (xcd >> 1) * 4 + j;
      if (unit < 14) role_cs((unit & 1) ? wu : wg, ws, tag, unit, (unit >> 1) * 2 + (xcd & 1), w, lane);
      else if (b == 31) {
        poll_tags(ws, SEEN_OFF + (w * 64 + lane) * 8, true, tag);
        BAR();
        if (w == 0 && lane == 0) set_tag(ws, EPOCH_OFF, tag);
      }
    }
    role_down(x, wd, wo, out, ws, tag, xcd * 4 + (j >> 3), j & 7, w, lane TS_ARG);
    TS_DUMP(1);
    KEEP(tch);
  }
}

extern "C" void kernel_launch(void* const* d_in, const int* in_sizes, int n_in, void* d_out, int out_size, void* d_ws, size_t ws_size, hipStream_t stream) {
  (void)hipFuncSetAttribute((const void*)k_all, hipFuncAttributeMaxDynamicSharedMemorySize, LDS_BYTES);
  k_all<<<GRID, 512, LDS_BYTES, stream>>>((const u16*)d_in[0], (const u16*)d_in[1], (const u16*)d_in[2], (const u16*)d_in[3], (const u16*)d_in[4],
                                         (const u16*)d_in[6], (const u16*)d_in[7], (const u16*)d_in[8], (u16*)d_out, (char*)d_ws);
}
```

```cpp
#include <hip/hip_runtime.h>
#include <stdint.h>
#pragma clang diagnostic ignored "-Wint-to-pointer-cast"

typedef unsigned short u16;
typedef unsigned long long u64;
typedef __bf16 bf16x2 __attribute__((ext_vector_type(2)));
typedef __bf16 bf16x8 __attribute__((ext_vector_type(8)));
typedef float f32x2 __attribute__((ext_vector_type(2)));
typedef float f32x4 __attribute__((ext_vector_type(4)));
typedef float f32x16 __attribute__((ext_vector_type(16)));
typedef unsigned u32x2 __attribute__((ext_vector_type(2)));
typedef unsigned u32x4 __attribute__((ext_vector_type(4)));
typedef short s16x4 __attribute__((ext_vector_type(4)));
typedef __attribute__((address_space(1))) u64 gu64;
#define LDSP(T) __attribute__((address_space(3))) T*
#define DI __device__ __forceinline__
#define RLX_AGENT __ATOMIC_RELAXED, __HIP_MEMORY_SCOPE_AGENT

constexpr int S_ = 2048, D_ = 512, F_ = 1792;
constexpr unsigned KF_OFF = 65536;
constexpr unsigned VF_OFF = KF_OFF + S_ * 16 * 2;
constexpr unsigned OF_OFF = VF_OFF + S_ * 16 * 2;
constexpr unsigned HF_OFF = OF_OFF + S_ * D_ * 2;
constexpr unsigned ST_OFF = HF_OFF + S_ * F_ * 2;
constexpr unsigned CS_OFF = ST_OFF + S_ * 8;
constexpr unsigned PAY_END = CS_OFF + 2 * F_ * 4;
constexpr unsigned CTL_OFF = 0;
constexpr unsigned EPOCH_OFF = CTL_OFF;
constexpr unsigned KVF_OFF = CTL_OFF + 4096;
constexpr unsigned SEEN_OFF = CTL_OFF + 8192;
constexpr unsigned CSF_OFF = CTL_OFF + 12288;
constexpr unsigned OFL_OFF = CTL_OFF + 32768;
constexpr unsigned HFL_OFF = CTL_OFF + 16384;
constexpr int LDS_BYTES = 80 * 1024;
constexpr float QSCALE = 0.25f * 1.4426950408889634f;
constexpr int SPIN_MAX = 40000;

DI f32x16 mfma(u32x4 a, u32x4 b, f32x16 c) {
  return __builtin_amdgcn_mfma_f32_32x32x16_bf16(__builtin_bit_cast(bf16x8, a), __builtin_bit_cast(bf16x8, b), c, 0, 0, 0);
}
DI unsigned cvtpk(float lo, float hi) { f32x2 v = {lo, hi}; bf16x2 b = __builtin_convertvector(v, bf16x2); return __builtin_bit_cast(unsigned, b); }
DI float bfr(float f) { return __uint_as_float(cvtpk(f, f) << 16); }
DI float dot2(unsigned a, unsigned b, float c) { return __builtin_amdgcn_fdot2_f32_bf16(__builtin_bit_cast(bf16x2, a), __builtin_bit_cast(bf16x2, b), c, false); }
DI u32x4 lds128(unsigned addr) { return *(LDSP(const u32x4))(addr); }
DI f32x4 lds128f(unsigned addr) { return *(LDSP(const f32x4))(addr); }
DI f32x2 lds64f(unsigned addr) { return *(LDSP(const f32x2))(addr); }
DI float lds32f(unsigned addr) { return *(LDSP(const float))(addr); }
DI void sts128(unsigned addr, u32x4 v) { *(LDSP(u32x4))(addr) = v; }
DI void sts128f(unsigned addr, f32x4 v) { *(LDSP(f32x4))(addr) = v; }
DI void sts64f(unsigned addr, f32x2 v) { *(LDSP(f32x2))(addr) = v; }
DI void sts32f(unsigned addr, float v) { *(LDSP(float))(addr) = v; }
DI u32x2 ldstr(unsigned addr) { return __builtin_bit_cast(u32x2, __builtin_amdgcn_ds_read_tr16_b64_v4i16((LDSP(s16x4))(addr))); }
DI u32x4 ldstr2(unsigned a0, unsigned a1) { u32x2 lo = ldstr(a0), hi = ldstr(a1); return (u32x4){lo[0], lo[1], hi[0], hi[1]}; }
typedef __amdgpu_buffer_rsrc_t rsrc_t;
DI rsrc_t mkrs(const void* p, unsigned bytes) { return __builtin_amdgcn_make_buffer_rsrc((void*)p, 0, (int)bytes, 0x00020000); }
#define BDMA_(MOD, rs, voff, ldsw, lconst, gconst) do { unsigned t_; \
  asm volatile("s_add_u32 m0, %2, %4\n\ts_mov_b32 %0, %5\n\ts_nop 2\n\tbuffer_load_dwordx4 %1, %3, %0 offen " MOD "lds" \
               : "=&s"(t_) : "v"(voff), "s"(ldsw), "s"(rs), "i"(lconst), "i"(gconst) : "memory", "scc"); } while (0)
#define BDMA(rs, voff, ldsw, lconst, gconst) BDMA_("", rs, voff, ldsw, lconst, gconst)
#define BDMA_SC1(rs, voff, ldsw, lconst, gconst) BDMA_("sc1 ", rs, voff, ldsw, lconst, gconst)
DI void bdma_rt_sc1(rsrc_t rs, unsigned voff, unsigned lds, unsigned goff) {
  lds = __builtin_amdgcn_readfirstlane(lds); goff = __builtin_amdgcn_readfirstlane(goff);
  asm volatile("s_mov_b32 m0, %1\n\ts_nop 3\n\tbuffer_load_dwordx4 %0, %2, %3 offen sc1 lds" :: "v"(voff), "s"(lds), "s"(rs), "s"(goff) : "memory");
}
DI void touch_lds(rsrc_t rs, unsigned voff) { asm volatile("s_mov_b32 m0, 0x13800\n\ts_nop 3\n\tbuffer_load_dword %0, %1, 0 offen lds" :: "v"(voff), "s"(rs) : "memory"); }
constexpr unsigned OOB = 0x7f000000u;
#define SBAR() __builtin_amdgcn_sched_barrier(0)
#define PIN(x) asm volatile("" : "+v"(x))
#define BAR() asm volatile("s_waitcnt lgkmcnt(0)\n\ts_barrier" ::: "memory")
#define DRAIN() asm volatile("s_waitcnt vmcnt(0)" ::: "memory")
template <int N> DI void waitv_bar() { asm volatile("s_waitcnt vmcnt(%0) lgkmcnt(0)\n\ts_barrier" :: "i"(N) : "memory"); }
template <int N> DI void waitv_bar_nl() { asm volatile("s_waitcnt vmcnt(%0)\n\ts_barrier" :: "i"(N) : "memory"); }
template <int C> DI float dppadd(float v) { return v + __uint_as_float(__builtin_amdgcn_update_dpp(0, __float_as_uint(v), C, 0xf, 0xf, true)); }
DI f32x16 zero16() { f32x16 z; for (int i = 0; i < 16; ++i) z[i] = 0.f; return z; }
DI float xhalf_add(float v) {
  auto r = __builtin_amdgcn_permlane32_swap(__float_as_uint(v), __float_as_uint(v), false, false);
  return __uint_as_float(r[0]) + __uint_as_float(r[1]);
}
DI float xhalf_max(float v) {
  auto r = __builtin_amdgcn_permlane32_swap(__float_as_uint(v), __float_as_uint(v), false, false);
  return fmaxf(__uint_as_float(r[0]), __uint_as_float(r[1]));
}
constexpr unsigned ONES2 = 0x3f803f80u;
DI float sum8(u32x4 v, float s) { s = dot2(v[0], ONES2, s); s = dot2(v[1], ONES2, s); s = dot2(v[2], ONES2, s); return dot2(v[3], ONES2, s); }
DI float sq8(u32x4 v, float s) { s = dot2(v[0], v[0], s); s = dot2(v[1], v[1], s); s = dot2(v[2], v[2], s); return dot2(v[3], v[3], s); }
DI u32x4 pack8f(f32x4 a, f32x4 b) { return (u32x4){cvtpk(a[0], a[1]), cvtpk(a[2], a[3]), cvtpk(b[0], b[1]), cvtpk(b[2], b[3])}; }
DI f32x4 q4(const f32x16& a, int g) { return (f32x4){a[4 * g], a[4 * g + 1], a[4 * g + 2], a[4 * g + 3]}; }
DI void st_sc1(char* ws, unsigned off, u32x4 v) {
  __amdgpu_buffer_rsrc_t rs = __builtin_amdgcn_make_buffer_rsrc(ws, 0, (int)PAY_END, 0x00020000);
  __builtin_amdgcn_raw_buffer_store_b128(v, rs, (int)off, 0, 16);
}
DI u64 load_tag(const char* ws) { u64 e; asm volatile("s_load_dwordx2 %0, %1, 0x0\n\ts_waitcnt lgkmcnt(0)" : "=s"(e) : "s"(ws) : "memory"); return e + 1; }
DI void set_tag(char* ws, unsigned off, u64 tag) { __hip_atomic_store((gu64*)(ws + off), tag, RLX_AGENT); }
DI void poll_tags(char* ws, unsigned off, bool active, u64 tag) {
#pragma unroll 1
  for (int it = 0; it < SPIN_MAX; ++it) {
    u64 v = tag;
    if (active) v = __hip_atomic_load((gu64*)(ws + off), RLX_AGENT);
    if (__all(v == tag)) break;
    __builtin_amdgcn_s_sleep(1);
  }
}

DI f32x2 ln_scalars(float s, float ss) {
  float m = s * (1.f / D_);
  float var = fmaxf(ss * (1.f / D_) - m * m, 0.f);
  float sd = bfr(__builtin_amdgcn_sqrtf(bfr(bfr(var) + 1e-5f)));
  return (f32x2){bfr(m), __builtin_amdgcn_rcpf(sd)};
}

constexpr int VT = 1120, KVS = 8192 + 8 * VT;
DI u32x4 role_q(const u16* x, const u16* wq, const char* ws, u64& tag, int rep, int rb, int cb, int w, int lane) {
  constexpr int NS = 5, SLOT = 16384;
  const int mt = w & 1, nt = (w >> 1) & 1, kh = w >> 2;
  const int hh = lane >> 5;
  const int drow = 8 * w + (lane >> 3), dpc = lane & 7;
  const unsigned xoff = (rb * 64 + drow) * 1024 + ((dpc ^ ((drow >> 1) & 7)) << 4);
  const unsigned woff = drow * 1024 + cb * 128 + ((dpc ^ (((drow >> 1) & 1) << 2)) << 4);
  const rsrc_t rx = mkrs(x, S_ * D_ * 2), rw = mkrs(wq, D_ * D_ * 2);
  const unsigned lw = w * 1024;
  u32x4 lx0, lx1, lx2, lq0, lq1, lq2;
#pragma unroll
  for (int j = 0; j < NS; ++j) BDMA(rx, xoff, lw, j * SLOT, j * 128);
  asm volatile("buffer_load_dwordx4 %0, %3, %4, 0 offen offset:640\n\tbuffer_load_dwordx4 %1, %3, %4, 0 offen offset:768\n\tbuffer_load_dwordx4 %2, %3, %4, 0 offen offset:896"
               : "=&v"(lx0), "=&v"(lx1), "=&v"(lx2) : "v"(xoff), "s"(rx) : "memory");
#pragma unroll
  for (int j = 0; j < NS; ++j) BDMA(rw, woff, lw, j * SLOT + 8192, j * 65536);
  {
    unsigned t0, t1, t2;
    asm volatile("s_mov_b32 %3, 0x50000\n\ts_mov_b32 %4, 0x60000\n\ts_mov_b32 %5, 0x70000\n\t"
                 "buffer_load_dwordx4 %0, %6, %7, %3 offen\n\tbuffer_load_dwordx4 %1, %6, %7, %4 offen\n\tbuffer_load_dwordx4 %2, %6, %7, %5 offen"
                 : "=&v"(lq0), "=&v"(lq1), "=&v"(lq2), "=&s"(t0), "=&s"(t1), "=&s"(t2) : "v"(woff), "s"(rw) : "memory");
  }
  const int xr = 32 * mt + (lane & 31), sw = (xr >> 1) & 7;
  const unsigned xa0 = xr * 128 + (((2 * kh + hh) ^ sw) << 4), xa1 = xr * 128 + (((2 * kh + 4 + hh) ^ sw) << 4);
  const int r4 = (lane & 15) >> 2, p4 = lane & 3, grp = (lane >> 4) & 1;
  const unsigned wa = 8192 + (8 * hh + r4) * 128 + ((nt ^ (r4 >> 1)) << 6) + (grp ^ kh) * 32 + p4 * 8 + kh * 2048;
  const unsigned sa = w * 1024 + lane * 16;
  f32x16 acc = zero16(), cs = zero16();
  const u32x4 ones = {ONES2, ONES2, ONES2, ONES2};
  u32x4 fw0[2], fw1[2], fx0[2], fx1[2];
#define Q_RD(i, sb) do { fw0[i] = ldstr2((sb) + wa, (sb) + wa + 512); fx0[i] = lds128((sb) + xa0); fw1[i] = ldstr2((sb) + wa + 4096, (sb) + wa + 4096 + 512); \
    fx1[i] = lds128((sb) + xa1); SBAR(); } while (0)
#define Q_MM(i) do { acc = mfma(fw0[i], fx0[i], acc); cs = mfma(fw0[i], ones, cs); acc = mfma(fw1[i], fx1[i], acc); cs = mfma(fw1[i], ones, cs); PIN(cs); PIN(acc); SBAR(); } while (0)
  tag = load_tag(ws);
  f32x2 st;
  {
    asm volatile("s_waitcnt vmcnt(8)" : "+v"(lx0), "+v"(lx1), "+v"(lx2) :: "memory");
    const u32x4 p0 = lds128(sa), p1 = lds128(SLOT + sa), p2 = lds128(2 * SLOT + sa), p3 = lds128(3 * SLOT + sa), p4_ = lds128(4 * SLOT + sa);
    float a1 = sum8(lx0, 0.f), a2 = sq8(lx0, 0.f), b1 = sum8(lx1, 0.f), b2 = sq8(lx1, 0.f);
    a1 = sum8(lx2, a1); a2 = sq8(lx2, a2); b1 = sum8(p0, b1); b2 = sq8(p0, b2);
    a1 = sum8(p1, a1); a2 = sq8(p1, a2); b1 = sum8(p2, b1); b2 = sq8(p2, b2);
    a1 = sum8(p3, a1); a2 = sq8(p3, a2); b1 = sum8(p4_, b1); b2 = sq8(p4_, b2);
    float s1 = a1 + b1, s2 = a2 + b2;
    s1 = dppadd<0xB1>(s1); s2 = dppadd<0xB1>(s2);
    s1 = dppadd<0x4E>(s1); s2 = dppadd<0x4E>(s2);
    s1 = dppadd<0x141>(s1); s2 = dppadd<0x141>(s2);
    st = ln_scalars(s1, s2);
    PIN(st);
  }
  waitv_bar<6>();
  Q_RD(0, 0); Q_RD(1, SLOT); Q_MM(0); Q_MM(1);
  waitv_bar<4>();
  Q_RD(0, 2 * SLOT); Q_RD(1, 3 * SLOT); Q_MM(0); Q_MM(1);
  waitv_bar<3>();
  Q_RD(0, 4 * SLOT);
  sts128(sa, lx0); sts128(SLOT + sa, lx1); sts128(2 * SLOT + sa, lx2);
  sts64f(3 * SLOT + drow * 8, st);
  SBAR();
  Q_MM(0);
  asm volatile("s_waitcnt vmcnt(0)" : "+v"(lq0), "+v"(lq1), "+v"(lq2) :: "memory");
  sts128(8192 + sa, lq0); sts128(SLOT + 8192 + sa, lq1); sts128(2 * SLOT + 8192 + sa, lq2);
  BAR();
  Q_RD(0, 0); Q_RD(1, SLOT);
  const f32x2 rs = lds64f(3 * SLOT + xr * 8);
  Q_MM(0); Q_RD(0, 2 * SLOT); Q_MM(1); Q_MM(0);
  sts128f(3 * KVS + sa, q4(acc, 2) - rs[0] * q4(cs, 2)); sts128f(4 * KVS + sa, q4(acc, 3) - rs[0] * q4(cs, 3));
  const f32x4 k0 = q4(acc, 0) - rs[0] * q4(cs, 0), k1 = q4(acc, 1) - rs[0] * q4(cs, 1);
  if (w == 0) poll_tags((char*)ws, KVF_OFF + rep * 512 + lane * 8, true, tag);
  BAR();
  const rsrc_t rws = mkrs(ws, PAY_END);
  const unsigned lv = w * VT;
#pragma unroll
  for (int c = 0; c < 3; ++c) { BDMA_SC1(rws, sa, lw, c * KVS, KF_OFF + c * 8192); BDMA_SC1(rws, sa, lv, c * KVS + 8192, VF_OFF + c * 8192); }
  const float rq = rs[1] * QSCALE;
  u32x4 qf = pack8f((k0 + lds128f(3 * KVS + (sa ^ 4096))) * rq, (k1 + lds128f(4 * KVS + (sa ^ 4096))) * rq);
  PIN(qf);
  return qf;
}

template <int WHICH> DI void kv_loop(f32x16& acc, float& cs, float& s1, float& s2, unsigned xa0, unsigned xa1, unsigned wa, unsigned sa) {
#pragma unroll
  for (int J = 0; J < 4; ++J) {
    if (J == 0) waitv_bar<6>(); else if (J == 1) waitv_bar<4>(); else if (J == 2) waitv_bar<2>(); else waitv_bar<0>();
    const unsigned sb = J * 16384;
    u32x4 w0 = ldstr2(sb + wa, sb + wa + 128), x0 = lds128(sb + xa0);
    u32x4 w1 = ldstr2(sb + wa + 2048, sb + wa + 2048 + 128), x1 = lds128(sb + xa1);
    u32x4 xs = lds128(sb + sa);
    SBAR();
    acc = WHICH ? mfma(x0, w0, acc) : mfma(w0, x0, acc); cs = sum8(w0, cs); PIN(cs); SBAR();
    acc = WHICH ? mfma(x1, w1, acc) : mfma(w1, x1, acc); cs = sum8(w1, cs); s1 = sum8(xs, s1); s2 = sq8(xs, s2); PIN(cs); PIN(s1); PIN(s2); SBAR();
  }
}
DI void role_kv(const u16* x, const u16* wk, const u16* wv, char* ws, u64& tag, int gmt, int w, int lane) {
  const int which = w & 1, kq = w >> 1, hh = lane >> 5;
  const int drow = 4 * w + (lane >> 4), dpc = lane & 15;
  const unsigned xoff = (gmt * 32 + drow) * 1024 + ((dpc ^ (drow & 15)) << 4);
  const rsrc_t rx = mkrs(x, S_ * D_ * 2), rw = mkrs(w < 4 ? wk : wv, D_ * 16 * 2);
  const unsigned woff = (w & 3) * 1024 + lane * 16, lw = w * 1024;
#pragma unroll
  for (int J = 0; J < 4; ++J) { BDMA(rx, xoff, lw, J * 16384, J * 256); BDMA(rw, woff, lw, J * 16384 + 8192, J * 4096); }
  const int xr = lane & 31;
  const unsigned xa0 = xr * 256 + (((2 * kq + hh) ^ (xr & 15)) << 4);
  const unsigned xa1 = xr * 256 + (((2 * (kq + 4) + hh) ^ (xr & 15)) << 4);
  const int r4 = (lane & 15) >> 2, p4 = lane & 3, grp = (lane >> 4) & 1;
  const unsigned wa = 8192 + (16 * kq + 8 * hh + r4) * 32 + grp * 4096 + p4 * 8;
  const unsigned sa = w * 1024 + lane * 16;
  f32x16 acc = zero16();
  float s1 = 0.f, s2 = 0.f, cs = 0.f;
  tag = load_tag(ws);
  if (which) kv_loop<1>(acc, cs, s1, s2, xa0, xa1, wa, sa); else kv_loop<0>(acc, cs, s1, s2, xa0, xa1, wa, sa);
  s1 = dppadd<0xB1>(s1); s2 = dppadd<0xB1>(s2);
  s1 = dppadd<0x4E>(s1); s2 = dppadd<0x4E>(s2);
  s1 = dppadd<0x141>(s1); s2 = dppadd<0x141>(s2);
  s1 = dppadd<0x140>(s1); s2 = dppadd<0x140>(s2);
  cs = xhalf_add(cs);
  const f32x2 st = ln_scalars(s1, s2);
  constexpr unsigned STB = 65536, CSB = 65536 + 512;
  if ((lane & 15) == 0) { sts32f(STB + drow * 4, st[0]); sts32f(STB + 128 + drow * 4, st[1]); }
  if (which == 0 && lane < 32) sts32f(CSB + (kq * 32 + lane) * 4, cs);
  const unsigned EX = w * 4096 + lane * 16;
  sts128f(EX, q4(acc, 0)); sts128f(EX + 1024, q4(acc, 1));
  if (which) { sts128f(EX + 2048, q4(acc, 2)); sts128f(EX + 3072, q4(acc, 3)); }
  BAR();
  if (w == 0) {
    f32x4 a[8], c[8];
#pragma unroll
    for (int k = 0; k < 4; ++k) {
      const unsigned e = k * 8192 + lane * 16, ca = CSB + (k * 32 + 4 * hh) * 4;
      a[2 * k] = lds128f(e); a[2 * k + 1] = lds128f(e + 1024); c[2 * k] = lds128f(ca); c[2 * k + 1] = lds128f(ca + 32);
    }
    const float m = lds32f(STB + xr * 4), r = lds32f(STB + 128 + xr * 4);
    SBAR();
    const f32x4 a0 = (a[0] + a[2]) + (a[4] + a[6]), a1 = (a[1] + a[3]) + (a[5] + a[7]), c0 = (c[0] + c[2]) + (c[4] + c[6]), c1 = (c[1] + c[3]) + (c[5] + c[7]);
    st_sc1(ws, KF_OFF + gmt * 1024 + lane * 16, pack8f((a0 - m * c0) * r, (a1 - m * c1) * r));
    DRAIN();
  } else if (w & 1 & ~(w >> 2)) {
    const int s = w >> 1;
    f32x4 a[8]; float c[4];
#pragma unroll
    for (int k = 0; k < 4; ++k) {
      const unsigned e = k * 8192 + 4096 + s * 2048 + lane * 16;
      a[2 * k] = lds128f(e); a[2 * k + 1] = lds128f(e + 1024); c[k] = lds32f(CSB + (k * 32 + xr) * 4);
    }
    const unsigned sp = STB + (16 * s + 4 * hh) * 4;
    const f32x4 m0 = lds128f(sp), m1 = lds128f(sp + 32), r0 = lds128f(sp + 128), r1 = lds128f(sp + 160);
    SBAR();
    const f32x4 a0 = (a[0] + a[2]) + (a[4] + a[6]), a1 = (a[1] + a[3]) + (a[5] + a[7]);
    const float cc = (c[0] + c[1]) + (c[2] + c[3]);
    st_sc1(ws, xr >= 16 ? VF_OFF + gmt * 1024 + s * 512 + hh * 256 + (lane & 15) * 16 : OOB, pack8f((a0 - m0 * cc) * r0, (a1 - m1 * cc) * r1));
    DRAIN();
  } else if (w == 2) {
    const f32x2 m = lds64f(STB + (lane & 15) * 8), r = lds64f(STB + 128 + (lane & 15) * 8);
    if (lane < 16) st_sc1(ws, ST_OFF + gmt * 256 + lane * 16, __builtin_bit_cast(u32x4, (f32x4){m[0], r[0], m[1], r[1]}));
    DRAIN();
  }
  BAR();
  if (w == 0 && lane < 8) set_tag(ws, KVF_OFF + lane * 512 + gmt * 8, tag);
}

DI void role_cs(const u16* wm_, char* ws, u64 tag, int unit, int cb, int w, int lane) {
  const int tid = w * 64 + lane;
  const char* src = (const char*)wm_ + (size_t)(tid >> 4) * (F_ * 2) + cb * 256 + (tid & 15) * 16;
  u32x4 v[16];
#pragma unroll
  for (int i = 0; i < 16; ++i) v[i] = *(const u32x4*)(src + (size_t)i * 32 * (F_ * 2));
  float a[8] = {0, 0, 0, 0, 0, 0, 0, 0};
  unsigned sel_lo = 0x00003f80u;
  asm volatile("" : "+s"(sel_lo));
#pragma unroll
  for (int i = 0; i < 16; ++i)
#pragma unroll
    for (int k = 0; k < 4; ++k) { a[2 * k] = dot2(v[i][k], sel_lo, a[2 * k]); a[2 * k + 1] = dot2(v[i][k], 0x3f800000u, a[2 * k + 1]); }
  sts128f(tid * 32, (f32x4){a[0], a[1], a[2], a[3]}); sts128f(tid * 32 + 16, (f32x4){a[4], a[5], a[6], a[7]});
  BAR();
  if (w == 0) {
    f32x4 t = {0, 0, 0, 0};
#pragma unroll
    for (int i = 0; i < 16; ++i) t += lds128f((2 * i + (lane >> 5)) * 512 + (lane & 31) * 16);
#pragma unroll
    for (int i = 0; i < 4; ++i) t[i] = xhalf_add(t[i]);
    if (lane < 32) st_sc1(ws, CS_OFF + ((unit & 1) * F_ + cb * 128) * 4 + lane * 16, __builtin_bit_cast(u32x4, t));
    DRAIN();
    if (lane == 0) set_tag(ws, CSF_OFF + ((unit & 1) * 14 + cb) * 128, tag);
  }
  BAR();
}

DI void role_up(const u16* x, const u16* wg, const u16* wu, char* ws, u64& tag, bool need_tag, int rb, int cb, int rep, int w, int lane) {
  constexpr int NS = 3, SLOT = 24576;
  const int wm = w & 1, wn = w >> 1, hh = lane >> 5;
  const int arow = 16 * w + (lane >> 2);
  const unsigned aoff = (rb * 128 + arow) * 1024 + (((lane & 3) ^ ((arow >> 2) & 3)) << 4);
  const int brow = 4 * w + (lane >> 4);
  const unsigned boff = brow * (F_ * 2) + cb * 256 + (((lane & 15) ^ ((brow & 3) << 2)) << 4);
  const rsrc_t rx = mkrs(x, S_ * D_ * 2), rg = mkrs(wg, D_ * F_ * 2), ru = mkrs(wu, D_ * F_ * 2);
  const unsigned lw = w * 1024;
#pragma unroll
  for (int s = 0; s < NS; ++s) { BDMA(rx, aoff, lw, s * SLOT, s * 64); BDMA(rg, boff, lw, s * SLOT + 8192, s * 32 * F_ * 2); BDMA(ru, boff, lw, s * SLOT + 16384, s * 32 * F_ * 2); }
  const int r4 = (lane & 15) >> 2, p4 = lane & 3, grp = (lane >> 4) & 1;
  const unsigned pa = 8192 + (8 * hh + r4) * 256 + ((wn ^ r4) << 6) + grp * 32 + p4 * 8;
  const int xr = 64 * wm + (lane & 31), sw = (xr >> 2) & 3;
  const unsigned xa0 = xr * 64 + ((hh ^ sw) << 4), xa1 = xr * 64 + (((2 + hh) ^ sw) << 4);
  constexpr unsigned STB = NS * SLOT, CSB = STB + 1024;
  f32x16 ap0 = zero16(), ap1 = zero16(), aq0 = zero16(), aq1 = zero16();
  u32x4 Ax0, Ax1, Ap, Aq, Bx0, Bx1, Bp, Bq;
  if (need_tag) tag = load_tag(ws);
  waitv_bar<6>();
  if (w == 1 && lane == 0) set_tag(ws, SEEN_OFF + blockIdx.x * 8, tag);
#define UP_RD(X0, X1, P, Q, pp, xx) do { P = ldstr2((pp), (pp) + 1024); X0 = lds128(xx); X1 = lds128((xx) + 2048); Q = ldstr2((pp) + 8192, (pp) + 8192 + 1024); } while (0)
#define UP_DM(rs, vo, lc, so) asm volatile("s_add_u32 m0, %1, %4\n\ts_nop 0\n\tbuffer_load_dwordx4 %0, %2, %3 offen lds" :: "v"(vo), "s"(lw), "s"(rs), "s"(so), "i"(lc) : "memory", "scc")
#define UP_STAGE(sb, sn) do { \
    UP_RD(Bx0, Bx1, Bp, Bq, (sb) + pa + 4096, (sb) + xa1); SBAR(); \
    ap0 = mfma(Ap, Ax0, ap0); ap1 = mfma(Ap, Ax1, ap1); aq0 = mfma(Aq, Ax0, aq0); aq1 = mfma(Aq, Ax1, aq1); SBAR(); \
    waitv_bar<3>();        \
    UP_RD(Ax0, Ax1, Ap, Aq, (sn) + pa, (sn) + xa0); SBAR(); \
    ap0 = mfma(Bp, Bx0, ap0); SBAR(); UP_DM(rx, aoff, (sb), ka); \
    ap1 = mfma(Bp, Bx1, ap1); SBAR(); UP_DM(rg, boff, (sb) + 8192, kw); \
    aq0 = mfma(Bq, Bx0, aq0); SBAR(); UP_DM(ru, boff, (sb) + 16384, kw); \
    aq1 = mfma(Bq, Bx1, aq1); asm volatile("" : "+v"(ap0), "+v"(ap1), "+v"(aq0), "+v"(aq1)); SBAR(); \
    ka += 64; kw += 32 * F_ * 2; } while (0)
  UP_RD(Ax0, Ax1, Ap, Aq, pa, xa0);
  unsigned ka = 3 * 64, kw = 3 * (32 * F_ * 2);
  __builtin_amdgcn_s_setprio(0);
#pragma unroll 1
  for (int s = 0; s < 5; ++s) {
    UP_STAGE(0, SLOT); UP_STAGE(SLOT, 2 * SLOT); UP_STAGE(2 * SLOT, 0);
    if (s == 3 && w == 0) {
      __builtin_amdgcn_s_setprio(3);
      poll_tags(ws, lane < 4 ? KVF_OFF + rep * 512 + (rb * 4 + lane) * 8 : CSF_OFF + ((lane - 4) * 14 + cb) * 128, lane < 6, tag);
      const rsrc_t rws = mkrs(ws, PAY_END);
      BDMA_SC1(rws, rb * 1024 + lane * 16, lw, STB, ST_OFF);
      BDMA_SC1(rws, cb * 512 + (lane >> 5) * (F_ * 4) + (lane & 31) * 16, lw, CSB, CS_OFF);
      __builtin_amdgcn_s_setprio(0);
    }
  }
  UP_RD(Bx0, Bx1, Bp, Bq, pa + 4096, xa1);
  ap0 = mfma(Ap, Ax0, ap0); ap1 = mfma(Ap, Ax1, ap1); aq0 = mfma(Aq, Ax0, aq0); aq1 = mfma(Aq, Ax1, aq1);
  ap0 = mfma(Bp, Bx0, ap0); ap1 = mfma(Bp, Bx1, ap1); aq0 = mfma(Bq, Bx0, aq0); aq1 = mfma(Bq, Bx1, aq1);
  waitv_bar<0>();
  __builtin_amdgcn_s_setprio(2);
  constexpr float GA = -2.f * 0.7978845608028654f * 1.4426950408889634f, GB = GA * 0.044715f;
  const unsigned cpa = CSB + (wn * 32 + 4 * hh) * 4;
  f32x4 cg[4], cu[4];
#pragma unroll
  for (int g = 0; g < 4; ++g) { cg[g] = lds128f(cpa + g * 32); cu[g] = lds128f(cpa + 512 + g * 32); }
  const f32x2 rsm[2] = {lds64f(STB + xr * 8), lds64f(STB + (xr + 32) * 8)};
  SBAR();
#pragma unroll
  for (int m = 0; m < 2; ++m) {
    const f32x16& P = m ? ap1 : ap0; const f32x16& Q = m ? aq1 : aq0;
    const f32x2 rs = rsm[m];
    const float nm = -rs[0], r2 = rs[1] * rs[1], ca = rs[1] * GA, cbb = r2 * rs[1] * GB, ir2 = __builtin_amdgcn_rcpf(r2);
    u32x4 o[2];
#pragma unroll
    for (int g = 0; g < 4; ++g) {
#pragma unroll
      for (int i = 0; i < 2; ++i) {
        const f32x2 gg = __builtin_elementwise_fma((f32x2){cg[g][2 * i], cg[g][2 * i + 1]}, (f32x2){nm, nm}, (f32x2){P[4 * g + 2 * i], P[4 * g + 2 * i + 1]});
        const f32x2 uu = __builtin_elementwise_fma((f32x2){cu[g][2 * i], cu[g][2 * i + 1]}, (f32x2){nm, nm}, (f32x2){Q[4 * g + 2 * i], Q[4 * g + 2 * i + 1]});
        const f32x2 z = gg * __builtin_elementwise_fma(gg * gg, (f32x2){cbb, cbb}, (f32x2){ca, ca});
        const f32x2 e = {__builtin_amdgcn_exp2f(z[0]), __builtin_amdgcn_exp2f(z[1])};
        const f32x2 dn = __builtin_elementwise_fma(e, (f32x2){ir2, ir2}, (f32x2){ir2, ir2});
        const f32x2 h = gg * uu * (f32x2){__builtin_amdgcn_rcpf(dn[0]), __builtin_amdgcn_rcpf(dn[1])};
        o[g >> 1][(g & 1) * 2 + i] = cvtpk(h[0], h[1]);
      }
    }
    const unsigned dst = HF_OFF + ((rb * 4 + wm * 2 + m) * 112 + cb * 8 + wn * 2) * 1024 + lane * 16;
    st_sc1(ws, dst, o[0]); st_sc1(ws, dst + 1024, o[1]);
  }
  DRAIN();
  if (lane == 0) set_tag(ws, HFL_OFF + ((rb * 8 + wm * 4 + wn) * 16 + cb) * 8, tag);
}

DI u32x4 ld_sc1(char* ws, unsigned off) { return __builtin_amdgcn_raw_buffer_load_b128(mkrs(ws, PAY_END), (int)off, 0, 16); }
DI void role_down(const u16* x, const u16* wd, const u16* wo, u16* out, char* ws, u64 tag, int rb, int cb, int w, int lane) {
  __builtin_amdgcn_s_setprio(3);
  const int mt = w >> 2, kq = w & 3, hh = lane >> 5;
  const rsrc_t rws = mkrs(ws, PAY_END), rd = mkrs(wd, F_ * D_ * 2), ro = mkrs(wo, D_ * D_ * 2);
  const int r4 = (lane & 15) >> 2, p4 = lane & 3, grp = (lane >> 4) & 1;
  const size_t go = (size_t)(rb * 64 + mt * 32 + (lane & 31)) * D_ + cb * 64 + kq * 16 + hh * 8;
  const u32x2 xl = *(const u32x2*)(x + go - hh * 4), xh = *(const u32x2*)(x + go - hh * 4 + 8);
  const unsigned lb = w * 8192;
  const unsigned hoff = ((rb * 2 + mt) * 112 + w) * 1024 + lane * 16, hoff1 = ((rb * 2 + (mt ^ 1)) * 112 + w) * 1024 + lane * 16;
  const unsigned boff = (w * 16 + (lane >> 3)) * 1024 + cb * 128 + (((lane & 7) ^ (((lane >> 4) & 1) << 2)) << 4);
  const unsigned wa0 = lb + (4 * hh + r4) * 128 + (((r4 >> 1) ^ (kq >> 1)) << 6) + (grp ^ (kq & 1)) * 32 + p4 * 8, wa1 = wa0 ^ 64;
  const unsigned xa = lb + lane * 16;
  f32x16 c00 = zero16(), c01, c10, c11;
  c00[0] = __uint_as_float(xl[0] << 16); c00[1] = __uint_as_float(xl[0] & 0xffff0000u); c00[2] = __uint_as_float(xl[1] << 16); c00[3] = __uint_as_float(xl[1] & 0xffff0000u);
  c00[4] = __uint_as_float(xh[0] << 16); c00[5] = __uint_as_float(xh[0] & 0xffff0000u); c00[6] = __uint_as_float(xh[1] << 16); c00[7] = __uint_as_float(xh[1] & 0xffff0000u);
  {
    const unsigned foff = HFL_OFF + ((rb * 4 + (w >> 1)) * 16 + (lane & 15)) * 8;
    unsigned have = 0;
    u32x4 h0[4], h1[4];
#define DN_ISSUE(i) do { \
      if (!((have >> (i)) & 1)) { \
        __builtin_amdgcn_s_setprio(3); \
        _Pragma("unroll 1") for (int it = 0; it < SPIN_MAX; ++it) { \
          const u64 v = __hip_atomic_load((gu64*)(ws + foff), RLX_AGENT); \
          have = (unsigned)__ballot(v == tag); \
          if ((have >> (i)) & 1) break; \
          __builtin_amdgcn_s_sleep(12); \
        } \
        __builtin_amdgcn_s_setprio(0); \
      } \
      { unsigned t_; asm volatile("s_mov_b32 %2, %6\n\ts_nop 3\n\tbuffer_load_dwordx4 %0, %3, %5, %2 offen sc1\n\tbuffer_load_dwordx4 %1, %4, %5, %2 offen sc1" \
          : "=&v"(h0[(i) & 3]), "=&v"(h1[(i) & 3]), "=&s"(t_) : "v"(hoff), "v"(hoff1), "s"(rws), "i"(HF_OFF + (i) * 8192) : "memory"); } \
      BDMA(rd, boff, lb, ((i) & 3) * 2048, (i) * 131072); BDMA(rd, boff, lb, ((i) & 3) * 2048 + 1024, (i) * 131072 + 8192); } while (0)
#define DN_WO(k) do { BDMA(ro, boff, lb, (((k) + 2) & 3) * 2048, (k) * 131072); BDMA(ro, boff, lb, (((k) + 2) & 3) * 2048 + 1024, (k) * 131072 + 8192); } while (0)
#define DN_STEP(i, CNT, NEXT) do { \
      asm volatile("s_waitcnt vmcnt(%2)" : "+v"(h0[(i) & 3]), "+v"(h1[(i) & 3]) : "i"(CNT) : "memory"); \
      const u32x4 u0 = ldstr2(wa0 + ((i) & 3) * 2048, wa0 + ((i) & 3) * 2048 + 1024), u1 = ldstr2(wa1 + ((i) & 3) * 2048, wa1 + ((i) & 3) * 2048 + 1024); \
      c00 = mfma(u0, h0[(i) & 3], c00); c01 = mfma(u1, h0[(i) & 3], (i) ? c01 : zero16()); \
      c10 = mfma(u0, h1[(i) & 3], (i) ? c10 : zero16()); c11 = mfma(u1, h1[(i) & 3], (i) ? c11 : zero16()); \
      asm volatile("" : "+v"(c00), "+v"(c01), "+v"(c10), "+v"(c11)); SBAR(); \
      NEXT; SBAR(); } while (0)
    PIN(c00);
    DN_ISSUE(0); DN_ISSUE(1); DN_ISSUE(2); DN_ISSUE(3);
    __builtin_amdgcn_s_setprio(0);
    DN_STEP(0, 12, DN_ISSUE(4)); DN_STEP(1, 12, DN_ISSUE(5)); DN_STEP(2, 12, DN_ISSUE(6)); DN_STEP(3, 12, DN_ISSUE(7));
    DN_STEP(4, 12, DN_ISSUE(8)); DN_STEP(5, 12, DN_ISSUE(9)); DN_STEP(6, 12, DN_ISSUE(10)); DN_STEP(7, 12, DN_ISSUE(11));
    DN_STEP(8, 12, DN_ISSUE(12)); DN_STEP(9, 12, DN_ISSUE(13));
    DN_STEP(10, 12, DN_WO(0)); DN_STEP(11, 10, DN_WO(1)); DN_STEP(12, 8, DN_WO(2)); DN_STEP(13, 6, DN_WO(3));
  }
  {
    const unsigned oo0 = OF_OFF + ((rb * 2 + mt) * 32 + w) * 1024 + lane * 16, oo1 = oo0 ^ 32768;
    DRAIN();
    u32x4 u0[2], u1[2];
    u0[0] = ldstr2(wa0 + 4096, wa0 + 5120); u1[0] = ldstr2(wa1 + 4096, wa1 + 5120); u0[1] = ldstr2(wa0 + 6144, wa0 + 7168); u1[1] = ldstr2(wa1 + 6144, wa1 + 7168);
    if (w == 0) {
#pragma unroll 1
      for (int sp = 0; sp < SPIN_MAX; ++sp) {
        const u64 v = __hip_atomic_load((gu64*)(ws + OFL_OFF + rb * 1024 + (lane & 7) * 128), RLX_AGENT);
        if (__all(v == tag)) break;
        __builtin_amdgcn_s_sleep(4);
      }
    }
    asm volatile("s_barrier" ::: "memory");
    __builtin_amdgcn_s_setprio(3);
    u32x4 of[8];
#pragma unroll
    for (int j = 0; j < 4; ++j) {
      of[2 * j] = __builtin_amdgcn_raw_buffer_load_b128(rws, (int)oo0, j * 8192, 16);
      of[2 * j + 1] = __builtin_amdgcn_raw_buffer_load_b128(rws, (int)oo1, j * 8192, 16);
    }
    SBAR();
#pragma unroll
    for (int j = 0; j < 4; ++j) {
      c10 = mfma(u0[j & 1], of[2 * j + 1], c10); c11 = mfma(u1[j & 1], of[2 * j + 1], c11); c00 = mfma(u0[j & 1], of[2 * j], c00); c01 = mfma(u1[j & 1], of[2 * j], c01);
      SBAR();
      if (j < 2) { u0[j] = ldstr2(wa0 + j * 2048, wa0 + j * 2048 + 1024); u1[j] = ldstr2(wa1 + j * 2048, wa1 + j * 2048 + 1024); }
    }
  }
#pragma unroll
  for (int g = 0; g < 4; ++g) { sts128f(xa + g * 1024, q4(c10, g)); sts128f(xa + 4096 + g * 1024, q4(c11, g)); }
  BAR();
  const unsigned eb = xa ^ 32768;
  f32x4 e[8];
#pragma unroll
  for (int g = 0; g < 8; ++g) e[g] = lds128f(eb + g * 1024);
  SBAR();
#pragma unroll
  for (int g = 2; g < 8; ++g) sts128f(eb + g * 1024, e[g] + (g < 4 ? q4(c00, g) : q4(c01, g - 4)));
  f32x4 k0 = e[0] + q4(c00, 0), k1 = e[1] + q4(c00, 1);
  BAR();
#pragma unroll
  for (int i = 1; i < 4; ++i) { const unsigned rbs = (eb ^ (i * 8192)) + i * 2048; e[2 * i] = lds128f(rbs); e[2 * i + 1] = lds128f(rbs + 1024); }
  SBAR();
  k0 = (k0 + e[2]) + (e[4] + e[6]); k1 = (k1 + e[3]) + (e[5] + e[7]);
#pragma unroll
  for (int i = 0; i < 4; ++i) {
    auto r = __builtin_amdgcn_permlane32_swap(__float_as_uint(k0[i]), __float_as_uint(k1[i]), false, false);
    k0[i] = __uint_as_float(r[0]); k1[i] = __uint_as_float(r[1]);
  }
  *(u32x4*)(out + go) = pack8f(k0, k1);
}

DI u32x4 pack8(const f32x16& p, int b) {
  return (u32x4){cvtpk(p[b], p[b + 1]), cvtpk(p[b + 2], p[b + 3]), cvtpk(p[b + 4], p[b + 5]), cvtpk(p[b + 6], p[b + 7])};
}
DI void role_attn(char* ws, const u16* wd, const u16* wo, u64 tag, int tu, const u32x4 qf, int rb, int cb, int w, int lane) {
  constexpr int SLOT = KVS;
  const int gmt = rb * 2 + (w & 1), head = cb * 4 + ((w >> 1) & 1) * 2 + (w >> 2);
  const int hh = lane >> 5, d = lane & 31;
  const unsigned loff = lane * 16;
  const rsrc_t rws = mkrs(ws, PAY_END);
  if (w == 2) *(LDSP(unsigned))0x13900 = 0;
  if (w < 2) {
    const int t = lane >> 1; const unsigned v = (lane & 1) ? 0u : ONES2;
    sts128((t >> 3) * SLOT + 8192 + (t & 7) * VT + 1024 + w * 64 + (lane & 1) * 16, (u32x4){v, v, v, v});
  }
  const unsigned va0 = 8192 + (d < 16 ? hh * 256 + d * 16 : 1024 + ((d >= 20 && d < 28) ? 64 : 0) + ((d == 16 || d == 20) ? 0 : 16));
  const unsigned va1 = va0 + (d < 16 ? 512 : 0);
  f32x16 o = zero16(), negm, sA, sB;
  waitv_bar<4>();
  u32x4 kf, v0, v1, pl, ph;
  {
    u32x4 k0 = lds128(loff);
    kf = lds128(loff + 1024);
    v0 = lds128(va0);
    v1 = (u32x4){0, 0, 0, 0}; ph = (u32x4){0, 0, 0, 0};
    f32x16 s = mfma(k0, qf, zero16());
#define MX3(a, b, c) fmaxf(fmaxf(a, b), c)
    float m = fmaxf(MX3(MX3(s[0], s[1], s[2]), MX3(s[3], s[4], s[5]), MX3(s[6], s[7], s[8])), MX3(MX3(s[9], s[10], s[11]), MX3(s[12], s[13], s[14]), s[15]));
    m = xhalf_max(m);
#pragma unroll
    for (int i = 0; i < 16; ++i) { negm[i] = -m; sA[i] = s[i] - m; }
  }
  if (w == 7 && lane == 0) set_tag(ws, SEEN_OFF + blockIdx.x * 8, tag);
#define EX(i) C[i] = __builtin_amdgcn_exp2f(C[i])
#define ATILE(CUR, NXT, KN, V1A, V0N) do { f32x16& C = CUR; \
    NXT = mfma(kf, qf, negm); SBAR(); kf = lds128(KN); SBAR(); \
    EX(0); EX(1); EX(2); EX(3); pl[0] = cvtpk(C[0], C[1]); EX(4); EX(5); SBAR(); \
    o = mfma(v1, ph, o); SBAR(); v1 = lds128(V1A); SBAR(); \
    EX(6); pl[1] = cvtpk(C[2], C[3]); EX(7); pl[2] = cvtpk(C[4], C[5]); EX(8); pl[3] = cvtpk(C[6], C[7]); SBAR(); EX(9); EX(10); SBAR(); \
    o = mfma(v0, pl, o); SBAR(); v0 = lds128(V0N); SBAR(); \
    EX(11); EX(12); EX(13); EX(14); EX(15); SBAR(); ph[0] = cvtpk(C[8], C[9]); ph[1] = cvtpk(C[10], C[11]); ph[2] = cvtpk(C[12], C[13]); ph[3] = cvtpk(C[14], C[15]); SBAR(); \
  } while (0)
  unsigned sb = 0, so = 3 * 8192 + w * 1024;
#pragma unroll 1
  for (int c = 0; c < 8; ++c) {
    if (c == 2) {
      touch_lds(mkrs(wd, F_ * D_ * 2), lane < 56 ? (tu * 56 + lane) * 128 : OOB);
      touch_lds(mkrs(wo, D_ * D_ * 2), lane < 16 ? (tu * 16 + lane) * 128 : OOB);
    }
    const unsigned sn = sb == 3 * SLOT ? 0 : sb + SLOT, sf = sb == 0 ? 3 * SLOT : sb - SLOT;
    unsigned ka = sb + loff, kn = sn + loff, a0 = sb + va0, a1 = sb + va1, an = sn + va0;
    asm volatile("" : "+v"(ka), "+v"(kn), "+v"(a0), "+v"(a1), "+v"(an));
    SBAR();
    ATILE(sA, sB, ka + 2 * 1024, a1 + 0 * VT, a0 + 1 * VT);
    ATILE(sB, sA, ka + 3 * 1024, a1 + 1 * VT, a0 + 2 * VT);
    __builtin_amdgcn_s_setprio(1);
    ATILE(sA, sB, ka + 4 * 1024, a1 + 2 * VT, a0 + 3 * VT);
    ATILE(sB, sA, ka + 5 * 1024, a1 + 3 * VT, a0 + 4 * VT);
    waitv_bar_nl<2>();
    bdma_rt_sc1(rws, loff, sf + w * 1024, KF_OFF + so);
    bdma_rt_sc1(rws, loff, sf + 8192 + w * VT, VF_OFF + so);
    so = c < 4 ? so + 8192 : OOB;
    __builtin_amdgcn_s_setprio(3);
    SBAR();
    ATILE(sA, sB, ka + 6 * 1024, a1 + 4 * VT, a0 + 5 * VT);
    ATILE(sB, sA, ka + 7 * 1024, a1 + 5 * VT, a0 + 6 * VT);
    ATILE(sA, sB, kn, a1 + 6 * VT, a0 + 7 * VT);
    __builtin_amdgcn_s_setprio(2);
    ATILE(sB, sA, kn + 1024, a1 + 7 * VT, an);
    sb = sn;
  }
  __builtin_amdgcn_s_setprio(3);
  o = mfma(v1, ph, o);
  float l = o[8];
  bool bad = !(l <= 1.8446744e19f);
#pragma unroll
  for (int i = 0; i < 8; ++i) bad |= !(__builtin_fabsf(o[i]) <= 3.0e38f);
  if (__any(bad)) {
    o = zero16();
    float m = -__builtin_inff();
    const unsigned cv = (d == 16 || d == 20) ? ONES2 : 0u;
    for (int t = 0; t < 64; ++t) {
      u32x4 kq = ld_sc1(ws, KF_OFF + t * 1024 + loff);
      u32x4 u0 = ld_sc1(ws, VF_OFF + t * 1024 + hh * 256 + (d & 15) * 16), u1 = ld_sc1(ws, VF_OFF + t * 1024 + 512 + hh * 256 + (d & 15) * 16);
      if (d >= 16) { u0 = (u32x4){cv, cv, cv, cv}; u1 = u0; }
      f32x16 s = mfma(kq, qf, zero16());
      float tm = fmaxf(s[0], s[1]);
#pragma unroll
      for (int i = 2; i < 16; i += 2) tm = fmaxf(fmaxf(tm, s[i]), s[i + 1]);
      tm = xhalf_max(tm);
      const float mn = fmaxf(m, tm);
      const float al = __builtin_amdgcn_exp2f(m - mn);
      m = mn;
      f32x16 p;
#pragma unroll
      for (int i = 0; i < 16; ++i) { p[i] = __builtin_amdgcn_exp2f(s[i] - mn); o[i] *= al; }
      o = mfma(u0, pack8(p, 0), o);
      o = mfma(u1, pack8(p, 8), o);
    }
    l = o[8];
  }
  const float inv = __builtin_amdgcn_rcpf(l);
  st_sc1(ws, OF_OFF + (gmt * 32 + head) * 1024 + loff, pack8f(q4(o, 0) * inv, q4(o, 1) * inv));
  DRAIN();
  if (lane == 0 && __hip_atomic_fetch_add((LDSP(unsigned))0x13900, 1u, __ATOMIC_RELAXED, __HIP_MEMORY_SCOPE_WORKGROUP) == 7) set_tag(ws, OFL_OFF + rb * 1024 + cb * 128, tag);
}

__global__ void __launch_bounds__(512, 4) k_all(const u16* x, const u16* wq, const u16* wk, const u16* wv, const u16* wo,
                                                const u16* wg, const u16* wu, const u16* wd, u16* out, char* ws) {
  const int lane = threadIdx.x & 63, w = __builtin_amdgcn_readfirstlane(threadIdx.x >> 6);
  const int b = blockIdx.x, xcd = b & 7, j = (b >> 3) & 31;
  u64 tag = 0;
  if (b >= 256) {
    __builtin_amdgcn_s_setprio(3);
    const int rb = xcd * 4 + (j >> 3), cb = j & 7;
    const u32x4 qf = role_q(x, wq, ws, tag, xcd, rb, cb, w, lane);
    role_attn(ws, wd, wo, tag, j * 8 + w, qf, rb, cb, w, lane);
  } else {
    __builtin_amdgcn_s_setprio(3);
    if (j < 8) {
      role_kv(x, wk, wv, ws, tag, xcd * 8 + j, w, lane);
    } else {
      if (j < 12 && w == 7) {
        u64 pc; asm volatile("s_getpc_b64 %0" : "=s"(pc));
        touch_lds(mkrs((const void*)(pc & ~127ull), 26624), ((j - 8) * 64 + lane) * 128);
      }
      for (int i = 0; i < 5; ++i) __builtin_amdgcn_s_sleep(8);
    }
    __builtin_amdgcn_s_setprio(0);
    if (j >= 4) role_up(x, wg, wu, ws, tag, j >= 8, (xcd >> 1) * 4 + (j & 3), (7 - (j >> 2)) * 2 + (xcd & 1), xcd, w, lane);
    else {
      __builtin_amdgcn_s_setprio(3);
      if (w == 1 && lane == 0) set_tag(ws, SEEN_OFF + b * 8, tag);
      const int unit = (xcd >> 1) * 4 + j;
      if (unit < 14) role_cs((unit & 1) ? wu : wg, ws, tag, unit, (unit >> 1) * 2 + (xcd & 1), w, lane);
      else if (b == 31) {
        for (int i = 0; i < 2; ++i) __builtin_amdgcn_s_sleep(127);
        poll_tags(ws, SEEN_OFF + (w * 64 + lane) * 8, true, tag);
        BAR();
        if (w == 0 && lane == 0) set_tag(ws, EPOCH_OFF, tag);
      }
    }
    role_down(x, wd, wo, out, ws, tag, xcd * 4 + (j >> 3), j & 7, w, lane);
  }
}

extern "C" void kernel_launch(void* const* d_in, const int* in_sizes, int n_in, void* d_out, int out_size, void* d_ws, size_t ws_size, hipStream_t stream) {
  (void)hipFuncSetAttribute((const void*)k_all, hipFuncAttributeMaxDynamicSharedMemorySize, LDS_BYTES);
  k_all<<<512, 512, LDS_BYTES, stream>>>((const u16*)d_in[0], (const u16*)d_in[1], (const u16*)d_in[2], (const u16*)d_in[3], (const u16*)d_in[4],
                                         (const u16*)d_in[6], (const u16*)d_in[7], (const u16*)d_in[8], (u16*)d_out, (char*)d_ws);
}
```
